```python
import torch

def _softmax(x, axis=-1):
    return torch.softmax(x, dim=axis)
def _gelu(x):
    return torch.nn.functional.gelu(x, approximate="tanh")
def _silu(x):
    return torch.nn.functional.silu(x)
def _sigmoid(x):
    return torch.sigmoid(x)
def _layernorm(x, axis=-1, eps=1e-5):
    m = x.mean(dim=axis, keepdim=True)
    v = x.var(dim=axis, keepdim=True, unbiased=False)
    return (x - m) / torch.sqrt(v + eps)
def _scatter_add(tgt, idx, src, axis=0):
    return tgt.clone().index_add_(axis, idx, src)
def _segment_sum(src, seg, n):
    out = torch.zeros((n,) + src.shape[1:], dtype=src.dtype)
    return out.index_add_(0, seg, src)
def _pad(x, pw):
    flat = tuple(v for pair in reversed(pw) for v in pair)
    return torch.nn.functional.pad(x, flat)

kernel_name = 'gated_mix__grouped_kv_mqa__glu_gated_bilinear__prompt'

def setup_inputs():

    _w = (lambda a: (lambda t: t.to(torch.bfloat16) if t.is_floating_point() else t)(torch.as_tensor(a)))
    def _A_setup():
        B, S, D, H = 1, 2048, 512, 32
        Dh = D // H
        return {
            "x": _w((lambda *s: torch.randn(*s, dtype=torch.bfloat16) * 0.05)(B, S, D)),
            "wq": _w((lambda *s: torch.randn(*s, dtype=torch.bfloat16) * 0.8)(D, H * Dh)),
            "wk": _w((lambda *s: torch.randn(*s, dtype=torch.bfloat16) * 0.8)(D, Dh)),
            "wv": _w((lambda *s: torch.randn(*s, dtype=torch.bfloat16) * 0.05)(D, Dh)),
            "wo": _w((lambda *s: torch.randn(*s, dtype=torch.bfloat16) * 0.05)(H * Dh, D)),
        }
    _A_in = _A_setup()

    def _B_setup():
        B, S, D, F = 1, 2048, 512, 1792
        return {
            "x": _w((lambda *s: torch.randn(*s, dtype=torch.bfloat16) * 0.05)(B, S, D)),
            "w_gate": _w((lambda *s: torch.randn(*s, dtype=torch.bfloat16) * 0.8)(D, F) * (D ** -0.5)),
            "w_up": _w((lambda *s: torch.randn(*s, dtype=torch.bfloat16) * 0.8)(D, F) * (D ** -0.5)),
            "w_down": _w((lambda *s: torch.randn(*s, dtype=torch.bfloat16) * 0.8)(F, D) * (F ** -0.5)),
        }
    _B_in = _B_setup()

    return {
        "A_x": _A_in["x"],
        "A_wq": _A_in["wq"],
        "A_wk": _A_in["wk"],
        "A_wv": _A_in["wv"],
        "A_wo": _A_in["wo"],
        "B_x": _B_in["x"],
        "B_w_gate": _B_in["w_gate"],
        "B_w_up": _B_in["w_up"],
        "B_w_down": _B_in["w_down"],
        "gate_w": _w((lambda *s: torch.randn(*s, dtype=torch.bfloat16) * 0.05)(512) * 16.0),
    }

def reference(A_x, A_wq, A_wk, A_wv, A_wo, B_x, B_w_gate, B_w_up, B_w_down, gate_w):
    def _A_ref(x, wq, wk, wv, wo):
        B, S, D = x.shape
        H = 32
        Dh = D // H
        q = (lambda x, *d: x.permute(*d))((x @ wq).reshape(B, S, H, Dh), 0, 2, 1, 3)
        k = x @ wk
        v = x @ wv
        scores = torch.einsum("bhqd,bkd->bhqk", q, k) * (Dh ** -0.5)
        p = _softmax(scores, axis=-1)
        o = torch.einsum("bhqk,bkd->bhqd", p, v)
        return (lambda x, *d: x.permute(*d))(o, 0, 2, 1, 3).reshape(B, S, H * Dh) @ wo

    def _B_ref(x, w_gate, w_up, w_down):
        g = torch.einsum("bsd,df->bsf", x, w_gate)
        u = torch.einsum("bsd,df->bsf", x, w_up)
        h = g * u
        return torch.einsum("bsf,fd->bsd", h, w_down)

    x = A_x
    g = _sigmoid((x * gate_w).sum(-1, keepdims=True))
    a = _A_ref(x, A_wq, A_wk, A_wv, A_wo)
    b = _B_ref(x, B_w_gate, B_w_up, B_w_down)
    return g * a + (1 - g) * b
```

```cpp
#include <hip/hip_runtime.h>
#include <hip/hip_bf16.h>
#include <stdint.h>

typedef __attribute__((ext_vector_type(8))) short bf16x8;
typedef __attribute__((ext_vector_type(4))) short s16x4;
typedef __attribute__((ext_vector_type(16))) float f32x16;
typedef __attribute__((ext_vector_type(4))) float f32x4;
typedef __attribute__((ext_vector_type(4))) unsigned u32x4;
typedef __attribute__((ext_vector_type(2))) float f32x2;
typedef __attribute__((ext_vector_type(2))) __bf16 bf16x2_t;
typedef __attribute__((address_space(3))) const char* lds_cptr;
typedef short v4i16_t __attribute__((ext_vector_type(4)));
#define DI __device__ __forceinline__
#define MFMA(a, b, c) __builtin_amdgcn_mfma_f32_32x32x16_bf16((a), (b), (c), 0, 0, 0)
#define SBAR() __builtin_amdgcn_sched_barrier(0)

constexpr int S_ = 2048, D_ = 512, H_ = 32, F_ = 1792;
constexpr float QSCALE = 0.25f * 1.4426950408889634f;

constexpr size_t WS_QF = 0;
constexpr size_t WS_OF = WS_QF + 2u * 1024 * 1024;
constexpr size_t WS_KV = WS_OF + 2u * 1024 * 1024;
constexpr size_t WS_G  = WS_KV + 256u * 1024;
constexpr size_t WS_BB = WS_G + 64u * 1024;
constexpr size_t WS_HF = WS_BB + 2u * 1024 * 1024;

DI unsigned pack_bf16(float lo, float hi) {
    f32x2 v = {lo, hi};
    bf16x2_t r = __builtin_convertvector(v, bf16x2_t);
    return __builtin_bit_cast(unsigned, r);
}
DI float bf_lo(unsigned u) { return __uint_as_float(u << 16); }
DI float bf_hi(unsigned u) { return __uint_as_float(u & 0xffff0000u); }

DI void glds16(const void* g, unsigned lds_base) {
    unsigned sv;
    asm volatile("s_mov_b32 %0, m0\n\ts_mov_b32 m0, %2\n\ts_nop 0\n\tglobal_load_lds_dwordx4 %1, off\n\ts_mov_b32 m0, %0"
                 : "=&s"(sv) : "v"(g), "s"(lds_base) : "memory");
}
#define WAIT_VM(N) asm volatile("s_waitcnt vmcnt(" #N ")" ::: "memory")
template <int N> DI void wait_vm_bar() { asm volatile("s_waitcnt vmcnt(%0) lgkmcnt(0)\n\ts_barrier" :: "i"(N) : "memory"); }
DI bf16x8 lds_b128(lds_cptr p) { return *(const __attribute__((address_space(3))) bf16x8*)p; }
DI s16x4 lds_tr(lds_cptr p) { return __builtin_bit_cast(s16x4, __builtin_amdgcn_ds_read_tr16_b64_v4i16((__attribute__((address_space(3))) v4i16_t*)p)); }
DI bf16x8 cat(s16x4 a, s16x4 b) { return __builtin_shufflevector(a, b, 0, 1, 2, 3, 4, 5, 6, 7); }
DI u32x4 pack8(const f32x16& x, int s, float sc) {
    u32x4 p;
    p[0] = pack_bf16(x[8 * s + 0] * sc, x[8 * s + 1] * sc); p[1] = pack_bf16(x[8 * s + 2] * sc, x[8 * s + 3] * sc);
    p[2] = pack_bf16(x[8 * s + 4] * sc, x[8 * s + 5] * sc); p[3] = pack_bf16(x[8 * s + 6] * sc, x[8 * s + 7] * sc);
    return p;
}

template <int KTOT, int NS, bool XFRAG, bool PERMN>
DI void gemm64(const char* __restrict__ xsrc, const char* __restrict__ W, int ldw_bytes, int tok0, int n0, char* lds, f32x16& acc) {
    const int tid = threadIdx.x, lane = tid & 63; const int w = __builtin_amdgcn_readfirstlane(tid >> 6);
    constexpr int NST = KTOT / 64, STB = 16384;
    const unsigned lds0 = (unsigned)(uintptr_t)lds;
    const char* xs; const char* ws;
    if (XFRAG) xs = xsrc + ((size_t)((tok0 >> 5) + (w >> 2)) * (KTOT / 16) + (w & 3)) * 1024 + lane * 16;
    else { int row = 8 * w + (lane >> 3); int ch = (lane & 7) ^ ((row >> 1) & 7); xs = xsrc + (size_t)(tok0 + row) * 1024 + ch * 16; }
    { int row = 8 * w + (lane >> 3); int ch = (lane & 7) ^ (((row >> 1) & 1) << 2); ws = W + (size_t)row * ldw_bytes + n0 * 2 + ch * 16; }
    const int xstep = XFRAG ? 4096 : 128; const int wstep = 64 * ldw_bytes;
    auto stage = [&](int s) {
        const unsigned b = lds0 + (s % NS) * STB + w * 1024;
        glds16(xs + (size_t)s * xstep, b);
        glds16(ws + (size_t)s * wstep, b + 8192);
    };
    const int ti = w & 1, ni = (w >> 1) & 1, kh = w >> 2, h = lane >> 5, r32 = lane & 31;
    int xoff[2], woff[2][2];
#pragma unroll
    for (int i = 0; i < 2; ++i) {
        const int ks = 2 * kh + i;
        if (XFRAG) xoff[i] = (ti * 4 + ks) * 1024 + lane * 16;
        else { int tok = ti * 32 + r32; xoff[i] = tok * 128 + (((2 * ks + h) ^ ((tok >> 1) & 7)) * 16); }
        const int g = (lane >> 4) & 1, q = (lane & 15) >> 2, p = lane & 3;
        const int pp = PERMN ? (((p & 1) << 1) | (p >> 1)) : p;
        const int col = ni * 32 + g * 16 + 4 * pp;
#pragma unroll
        for (int t = 0; t < 2; ++t) {
            const int row = XFRAG ? (16 * ks + 8 * t + 4 * h + q) : (16 * ks + 8 * h + 4 * t + q);
            woff[i][t] = 8192 + row * 128 + (((col >> 3) ^ (((row >> 1) & 1) << 2)) * 16) + (col & 7) * 2;
        }
    }
    acc = f32x16{};
#pragma unroll
    for (int s = 0; s < NS - 1 && s < NST; ++s) stage(s);
#pragma unroll 1
    for (int s = 0; s < NST; ++s) {
        const int rem = NST - 1 - s;
        if (rem >= NS - 2) wait_vm_bar<2 * (NS - 2)>();
        else if (NS > 3 && rem == 1) wait_vm_bar<2>();
        else if (NS > 4 && rem == 2) wait_vm_bar<4>();
        else if (NS > 5 && rem == 3) wait_vm_bar<6>();
        else if (NS > 6 && rem == 4) wait_vm_bar<8>();
        else if (NS > 7 && rem == 5) wait_vm_bar<10>();
        else wait_vm_bar<0>();
        if (s + NS - 1 < NST) stage(s + NS - 1);
        const lds_cptr b = (lds_cptr)lds + (s % NS) * STB;
#pragma unroll
        for (int i = 0; i < 2; ++i) {
            bf16x8 xf = lds_b128(b + xoff[i]);
            bf16x8 wf = cat(lds_tr(b + woff[i][0]), lds_tr(b + woff[i][1]));
            acc = MFMA(wf, xf, acc);
        }
    }
    wait_vm_bar<0>();
    float* red = (float*)lds;
    if (kh == 1) {
#pragma unroll
        for (int i = 0; i < 4; ++i) *(f32x4*)(red + ((w & 3) * 4 + i) * 256 + lane * 4) = (f32x4){acc[4 * i], acc[4 * i + 1], acc[4 * i + 2], acc[4 * i + 3]};
    }
    wait_vm_bar<0>();
    if (kh == 0) {
#pragma unroll
        for (int i = 0; i < 4; ++i) { f32x4 v = *(const f32x4*)(red + ((w & 3) * 4 + i) * 256 + lane * 4); acc[4 * i] += v[0]; acc[4 * i + 1] += v[1]; acc[4 * i + 2] += v[2]; acc[4 * i + 3] += v[3]; }
    }
}

DI void q_job(int tt64, int cg, const char* x, const char* wq, char* ws, char* lds) {
    f32x16 acc;
    gemm64<512, 4, false, false>(x, wq, 1024, tt64 * 64, cg * 64, lds, acc);
    const int tid = threadIdx.x, lane = tid & 63; const int w = __builtin_amdgcn_readfirstlane(tid >> 6);
    if (w < 4) {
        const int ti = w & 1, ni = w >> 1;
        char* dst = ws + WS_QF + ((size_t)(tt64 * 2 + ti) * 32 + cg * 4 + ni * 2) * 1024 + lane * 16;
        *(u32x4*)dst = pack8(acc, 0, QSCALE);
        *(u32x4*)(dst + 1024) = pack8(acc, 1, QSCALE);
    }
}
DI void kv_job(int tt64, const char* x, const char* wk, const char* wv, const char* gw, char* ws, char* lds) {
    const int tid = threadIdx.x, lane = tid & 63; const int w = __builtin_amdgcn_readfirstlane(tid >> 6);
    const int ti = w & 1, kq = w >> 1, h = lane >> 5, r32 = lane & 31;
    const unsigned short* wsel = (const unsigned short*)(r32 < 16 ? wv : wk);
    const int c16 = r32 & 15;
    f32x16 aT = {}, aN = {};
    const char* xr = x + (size_t)(tt64 * 64 + ti * 32 + r32) * 1024;
#pragma unroll
    for (int ks = 0; ks < 8; ++ks) {
        const int k0 = kq * 128 + ks * 16 + 8 * h;
        bf16x8 xf = *(const bf16x8*)(xr + k0 * 2);
        bf16x8 wf;
#pragma unroll
        for (int j = 0; j < 8; ++j) wf[j] = (short)wsel[(k0 + j) * 16 + c16];
        aT = MFMA(wf, xf, aT);
        aN = MFMA(xf, wf, aN);
    }
    float* red = (float*)lds;
#pragma unroll
    for (int i = 0; i < 16; ++i) { red[((w * 2 + 0) * 16 + i) * 64 + lane] = aT[i]; red[((w * 2 + 1) * 16 + i) * 64 + lane] = aN[i]; }
    __syncthreads();
    if (w < 2) {
#pragma unroll
        for (int q = 1; q < 4; ++q)
#pragma unroll
            for (int i = 0; i < 16; ++i) { aT[i] += red[(((w + 2 * q) * 2 + 0) * 16 + i) * 64 + lane]; aN[i] += red[(((w + 2 * q) * 2 + 1) * 16 + i) * 64 + lane]; }
        char* dst = ws + WS_KV + (size_t)(tt64 * 2 + w) * 3072 + lane * 16;
        *(u32x4*)dst = pack8(aT, 1, 1.0f);
        u32x4 v0 = pack8(aN, 0, 1.0f), v1 = pack8(aN, 1, 1.0f);
        if (r32 >= 16) { unsigned c = (r32 == 16 || r32 == 20) ? 0x3F803F80u : 0u; v0 = (u32x4){c, c, c, c}; v1 = v0; }
        *(u32x4*)(dst + 1024) = v0; *(u32x4*)(dst + 2048) = v1;
    }
    const u32x4 gv = *(const u32x4*)(gw + lane * 16);
#pragma unroll
    for (int t = 0; t < 8; ++t) {
        const int tok = tt64 * 64 + w * 8 + t;
        const u32x4 xv = *(const u32x4*)(x + (size_t)tok * 1024 + lane * 16);
        float s = 0.f;
#pragma unroll
        for (int i = 0; i < 4; ++i) s += bf_lo(xv[i]) * bf_lo(gv[i]) + bf_hi(xv[i]) * bf_hi(gv[i]);
#pragma unroll
        for (int o = 32; o >= 1; o >>= 1) s += __shfl_xor(s, o);
        if (lane == 0) ((float*)(ws + WS_G))[tok] = 1.0f / (1.0f + __expf(-s));
    }
}

DI void up_job(int rp, int cp, const char* x, const char* wg, const char* wu, char* ws, char* lds) {
    const int tid = threadIdx.x, lane = tid & 63; const int w = __builtin_amdgcn_readfirstlane(tid >> 6);
    constexpr int NS = 3, STB = 24576, NST = 16;
    const unsigned lds0 = (unsigned)(uintptr_t)lds;
    const char* xs; { int row = 16 * w + (lane >> 2); int ch = (lane & 3) ^ ((row >> 2) & 3); xs = x + (size_t)(rp * 128 + row) * 1024 + ch * 16; }
    const char *gs, *us; { int row = 4 * w + (lane >> 4); int ch = (lane & 15) ^ ((row & 3) << 2); size_t o = (size_t)row * (F_ * 2) + cp * 256 + ch * 16; gs = wg + o; us = wu + o; }
    auto stage = [&](int s) {
        const unsigned b = lds0 + (s % NS) * STB + w * 1024;
        glds16(xs + s * 64, b);
        glds16(gs + (size_t)s * 32 * F_ * 2, b + 8192);
        glds16(us + (size_t)s * 32 * F_ * 2, b + 16384);
    };
    const int th = w & 1, fg = w >> 1, h = lane >> 5, r32 = lane & 31;
    int xoff[2][2], woff[2][2];
#pragma unroll
    for (int ks = 0; ks < 2; ++ks) {
#pragma unroll
        for (int i = 0; i < 2; ++i) { int tok = th * 64 + i * 32 + r32; xoff[ks][i] = tok * 64 + (((2 * ks + h) ^ ((tok >> 2) & 3)) * 16); }
        const int g = (lane >> 4) & 1, q = (lane & 15) >> 2, p = lane & 3;
        const int col = fg * 32 + g * 16 + 4 * p;
#pragma unroll
        for (int t = 0; t < 2; ++t) { int row = 16 * ks + 8 * h + 4 * t + q; woff[ks][t] = 8192 + row * 256 + (((col >> 3) ^ ((row & 3) << 2)) * 16) + (col & 7) * 2; }
    }
    f32x16 ag[2] = {}, au[2] = {};
    stage(0); stage(1);
#pragma unroll 1
    for (int s = 0; s < NST; ++s) {
        if (s + 1 < NST) wait_vm_bar<3>(); else wait_vm_bar<0>();
        if (s + 2 < NST) stage(s + 2);
        const lds_cptr b = (lds_cptr)lds + (s % NS) * STB;
#pragma unroll
        for (int ks = 0; ks < 2; ++ks) {
            bf16x8 x0 = lds_b128(b + xoff[ks][0]), x1 = lds_b128(b + xoff[ks][1]);
            bf16x8 gf = cat(lds_tr(b + woff[ks][0]), lds_tr(b + woff[ks][1]));
            bf16x8 uf = cat(lds_tr(b + 8192 + woff[ks][0]), lds_tr(b + 8192 + woff[ks][1]));
            ag[0] = MFMA(gf, x0, ag[0]); ag[1] = MFMA(gf, x1, ag[1]);
            au[0] = MFMA(uf, x0, au[0]); au[1] = MFMA(uf, x1, au[1]);
        }
    }
#pragma unroll
    for (int i = 0; i < 2; ++i) {
        f32x16 hh = ag[i] * au[i];
        char* dst = ws + WS_HF + ((size_t)(rp * 4 + th * 2 + i) * 112 + cp * 8 + fg * 2) * 1024 + lane * 16;
        *(u32x4*)dst = pack8(hh, 0, 1.0f);
        *(u32x4*)(dst + 1024) = pack8(hh, 1, 1.0f);
    }
}

__global__ __launch_bounds__(512, 4) void k_qkv_up(const char* x, const char* wq, const char* wk, const char* wv, const char* gw,
                                                   const char* wg, const char* wu, char* ws) {
    extern __shared__ __attribute__((aligned(16))) char lds[];
    const int b = blockIdx.x;
    if (b < 224) { up_job(b / 14, b % 14, x, wg, wu, ws, lds); }
    else if (b < 256) { kv_job(b - 224, x, wk, wv, gw, ws, lds); }
    else { const int i = b - 256; q_job(i >> 3, i & 7, x, wq, ws, lds); }
}

constexpr int KVT = 3072, CHT = 8, CHB = CHT * KVT;
DI float xhalf_max(float m) { auto rr = __builtin_amdgcn_permlane32_swap(__float_as_uint(m), __float_as_uint(m), false, false); return fmaxf(__uint_as_float(rr[0]), __uint_as_float(rr[1])); }
DI float max16(const f32x16& s) {
    float a = fmaxf(fmaxf(s[0], s[1]), s[2]), b = fmaxf(fmaxf(s[3], s[4]), s[5]);
    a = fmaxf(fmaxf(a, s[6]), s[7]); b = fmaxf(fmaxf(b, s[8]), s[9]); a = fmaxf(fmaxf(a, s[10]), s[11]); b = fmaxf(fmaxf(b, s[12]), s[13]);
    a = fmaxf(fmaxf(a, s[14]), s[15]); return fmaxf(a, b);
}
DI u32x4 exp_pack(const f32x16& s, int o) {
    u32x4 p;
#pragma unroll
    for (int i = 0; i < 4; ++i) p[i] = pack_bf16(__builtin_amdgcn_exp2f(s[o + 2 * i]), __builtin_amdgcn_exp2f(s[o + 2 * i + 1]));
    return p;
}
__device__ __noinline__ f32x16 attn_exact(const char* kv, bf16x8 qf, int lane) {
    f32x16 o = {}; float m = -INFINITY;
#pragma unroll 1
    for (int t = 0; t < 64; ++t) {
        const char* p = kv + (size_t)t * KVT + lane * 16;
        bf16x8 kf = *(const bf16x8*)p, v0 = *(const bf16x8*)(p + 1024), v1 = *(const bf16x8*)(p + 2048);
        f32x16 s = MFMA(kf, qf, (f32x16){});
        const float mn = fmaxf(m, xhalf_max(max16(s)));
        const float al = __builtin_amdgcn_exp2f(m - mn);
        m = mn;
#pragma unroll
        for (int i = 0; i < 16; ++i) { o[i] *= al; s[i] -= mn; }
        o = MFMA(v0, __builtin_bit_cast(bf16x8, exp_pack(s, 0)), o);
        o = MFMA(v1, __builtin_bit_cast(bf16x8, exp_pack(s, 8)), o);
    }
    return o;
}
DI void attn_job(int blk, char* ws, char* lds) {
    const int tid = threadIdx.x, lane = tid & 63; const int w = __builtin_amdgcn_readfirstlane(tid >> 6);
    const int tt = blk >> 2, head = (blk & 3) * 8 + w;
    const unsigned lds0 = (unsigned)(uintptr_t)lds;
    const char* kvs = ws + WS_KV + w * KVT + lane * 16;
    auto stage = [&](int c) {
        const unsigned b = lds0 + (c % 3) * CHB + w * KVT;
        const char* s = kvs + (size_t)c * CHB;
        glds16(s, b); glds16(s + 1024, b + 1024); glds16(s + 2048, b + 2048);
    };
    stage(0);
    const bf16x8 qf = *(const bf16x8*)(ws + WS_QF + ((size_t)tt * 32 + head) * 1024 + lane * 16);
    stage(1);
    wait_vm_bar<3>();
    const lds_cptr lb = (lds_cptr)lds + lane * 16;
    f32x16 cn;
    { f32x16 s0 = MFMA(lds_b128(lb), qf, (f32x16){}); const float m = xhalf_max(max16(s0));
#pragma unroll
      for (int i = 0; i < 16; ++i) cn[i] = -m; }
    f32x16 o = {};
    f32x16 sc = MFMA(lds_b128(lb), qf, cn);
#pragma unroll 1
    for (int c = 0; c < 8; ++c) {
        const lds_cptr b = lb + (c % 3) * CHB;
        const lds_cptr bn = lb + ((c + 1) % 3) * CHB;
#pragma unroll
        for (int t = 0; t < CHT; ++t) {
            if (t == 4) { wait_vm_bar<0>(); if (c + 2 < 8) stage(c + 2); }
            const bf16x8 kn = lds_b128(t + 1 < CHT ? b + (t + 1) * KVT : bn);
            const bf16x8 v0 = lds_b128(b + t * KVT + 1024), v1 = lds_b128(b + t * KVT + 2048);
            const f32x16 sn = MFMA(kn, qf, cn);
            const u32x4 p0 = exp_pack(sc, 0);
            o = MFMA(v0, __builtin_bit_cast(bf16x8, p0), o);
            const u32x4 p1 = exp_pack(sc, 8);
            o = MFMA(v1, __builtin_bit_cast(bf16x8, p1), o);
            sc = sn;
        }
    }
    float mx = fabsf(o[0]);
#pragma unroll
    for (int i = 1; i < 8; ++i) mx = fmaxf(mx, fabsf(o[i]));
    const bool ok = (o[8] < 1.0e18f) && (o[8] > 0.5f) && (mx < 1.0e36f);
    if (__builtin_expect(!__all(ok), 0)) o = attn_exact(ws + WS_KV, qf, lane);
    const float inv = 1.0f / o[8];
    *(u32x4*)(ws + WS_OF + ((size_t)tt * 32 + head) * 1024 + lane * 16) = pack8(o, 0, inv);
}
__global__ __launch_bounds__(512, 4) void k_attn(char* ws) {
    extern __shared__ __attribute__((aligned(16))) char lds[];
    attn_job(blockIdx.x, ws, lds);
}

DI void down_job(int tile, const char* wd, char* ws, char* lds) {
    f32x16 acc;
    gemm64<1792, 4, true, true>(ws + WS_HF, wd, 1024, (tile >> 3) * 64, (tile & 7) * 64, lds, acc);
    const int tid = threadIdx.x, lane = tid & 63; const int w = __builtin_amdgcn_readfirstlane(tid >> 6);
    if (w < 4) {
        char* dst = ws + WS_BB + ((size_t)tile * 4 + w) * 2048 + lane * 16;
        *(u32x4*)dst = pack8(acc, 0, 1.0f);
        *(u32x4*)(dst + 1024) = pack8(acc, 1, 1.0f);
    }
}
__global__ __launch_bounds__(512, 4) void k_down(const char* wd, char* ws) {
    extern __shared__ __attribute__((aligned(16))) char lds[];
    down_job(blockIdx.x, wd, ws, lds);
}
__global__ __launch_bounds__(512, 2) void k_out(const char* wo, char* ws, char* out) {
    extern __shared__ __attribute__((aligned(16))) char lds[];
    const int tile = blockIdx.x;
    f32x16 acc;
    gemm64<512, 8, true, true>(ws + WS_OF, wo, 1024, (tile >> 3) * 64, (tile & 7) * 64, lds, acc);
    const int tid = threadIdx.x, lane = tid & 63; const int w = __builtin_amdgcn_readfirstlane(tid >> 6);
    if (w < 4) {
        const int ti = w & 1, ni = w >> 1, h = lane >> 5;
        const int tok = (tile >> 3) * 64 + ti * 32 + (lane & 31);
        const float g = ((const float*)(ws + WS_G))[tok];
        const char* bsrc = ws + WS_BB + ((size_t)tile * 4 + w) * 2048 + lane * 16;
#pragma unroll
        for (int c = 0; c < 2; ++c) {
            const u32x4 bv = *(const u32x4*)(bsrc + c * 1024);
            u32x4 r;
#pragma unroll
            for (int i = 0; i < 4; ++i)
                r[i] = pack_bf16(g * acc[8 * c + 2 * i] + (1.0f - g) * bf_lo(bv[i]), g * acc[8 * c + 2 * i + 1] + (1.0f - g) * bf_hi(bv[i]));
            *(u32x4*)(out + (size_t)tok * 1024 + ((tile & 7) * 64 + ni * 32 + c * 16 + 8 * h) * 2) = r;
        }
    }
}

extern "C" void kernel_launch(void* const* d_in, const int* in_sizes, int n_in, void* d_out, int out_size, void* d_ws, size_t ws_size, hipStream_t stream) {
    const char* x = (const char*)d_in[0]; const char* wq = (const char*)d_in[1]; const char* wk = (const char*)d_in[2]; const char* wv = (const char*)d_in[3];
    const char* wo = (const char*)d_in[4]; const char* wg = (const char*)d_in[6]; const char* wu = (const char*)d_in[7]; const char* wd = (const char*)d_in[8];
    const char* gw = (const char*)d_in[9];
    char* ws = (char*)d_ws;
    hipFuncSetAttribute((const void*)k_qkv_up, hipFuncAttributeMaxDynamicSharedMemorySize, 73728);
    hipFuncSetAttribute((const void*)k_attn, hipFuncAttributeMaxDynamicSharedMemorySize, 73728);
    hipFuncSetAttribute((const void*)k_down, hipFuncAttributeMaxDynamicSharedMemorySize, 65536);
    hipFuncSetAttribute((const void*)k_out, hipFuncAttributeMaxDynamicSharedMemorySize, 131072);
    k_qkv_up<<<512, 512, 73728, stream>>>(x, wq, wk, wv, gw, wg, wu, ws);
    k_attn<<<256, 512, 73728, stream>>>(ws);
    k_down<<<256, 512, 65536, stream>>>(wd, ws);
    k_out<<<256, 512, 131072, stream>>>(wo, ws, (char*)d_out);
}
```

```cpp
#include <hip/hip_runtime.h>
#include <hip/hip_bf16.h>
#include <stdint.h>

typedef __attribute__((ext_vector_type(8))) short bf16x8;
typedef __attribute__((ext_vector_type(4))) short s16x4;
typedef __attribute__((ext_vector_type(16))) float f32x16;
typedef __attribute__((ext_vector_type(4))) float f32x4;
typedef __attribute__((ext_vector_type(4))) unsigned u32x4;
typedef __attribute__((ext_vector_type(2))) float f32x2;
typedef __attribute__((ext_vector_type(2))) __bf16 bf16x2_t;
typedef __attribute__((address_space(3))) const char* lds_cptr;
typedef __attribute__((address_space(3))) char* lds_ptr;
typedef __attribute__((address_space(1))) unsigned gu32;
typedef short v4i16_t __attribute__((ext_vector_type(4)));
#define DI __device__ __forceinline__
#define MFMA(a, b, c) __builtin_amdgcn_mfma_f32_32x32x16_bf16((a), (b), (c), 0, 0, 0)
#define RLX_AGENT __ATOMIC_RELAXED, __HIP_MEMORY_SCOPE_AGENT

constexpr int F_ = 1792;
constexpr float QSCALE = 0.25f * 1.4426950408889634f;

constexpr size_t WS_CNT = 0;
constexpr size_t WS_G  = 8192;
constexpr size_t WS_KV = 65536;
constexpr size_t WS_QF = 1u * 1024 * 1024;
constexpr size_t WS_OF = 3u * 1024 * 1024;
constexpr size_t WS_BB = 5u * 1024 * 1024;
constexpr size_t WS_HF = 7u * 1024 * 1024;
constexpr size_t WS_DBG = 16u * 1024 * 1024;

#define STAMP(kid, i)

DI unsigned pack_bf16(float lo, float hi) { f32x2 v = {lo, hi}; bf16x2_t r = __builtin_convertvector(v, bf16x2_t); return __builtin_bit_cast(unsigned, r); }
DI float bf_lo(unsigned u) { return __uint_as_float(u << 16); }
DI float bf_hi(unsigned u) { return __uint_as_float(u & 0xffff0000u); }
DI void glds16(const void* g, unsigned lds_base) {
    unsigned sv;
    asm volatile("s_mov_b32 %0, m0\n\ts_mov_b32 m0, %2\n\ts_nop 0\n\tglobal_load_lds_dwordx4 %1, off\n\ts_mov_b32 m0, %0"
                 : "=&s"(sv) : "v"(g), "s"(__builtin_amdgcn_readfirstlane(lds_base)) : "memory");
}
DI u32x4 gld16_async(const void* g) { u32x4 r; asm volatile("global_load_dwordx4 %0, %1, off" : "=v"(r) : "v"(g) : "memory"); return r; }
DI unsigned gld4_async(const void* g) { unsigned r; asm volatile("global_load_dword %0, %1, off" : "=v"(r) : "v"(g) : "memory"); return r; }
DI void gst16_wt(void* g, u32x4 v) { asm volatile("global_store_dwordx4 %0, %1, off sc1\n\ts_nop 1" :: "v"(g), "v"(v) : "memory"); }
template <int N> DI void wait_vm_bar() { asm volatile("s_waitcnt vmcnt(%0) lgkmcnt(0)\n\ts_barrier" :: "i"(N) : "memory"); }
DI void lds_bar() { asm volatile("s_waitcnt lgkmcnt(0)\n\ts_barrier" ::: "memory"); }
DI bf16x8 lds_b128(lds_cptr p) { return *(const __attribute__((address_space(3))) bf16x8*)p; }
DI f32x4 lds_f4(lds_cptr p) { return *(const __attribute__((address_space(3))) f32x4*)p; }
DI void lds_st_f4(lds_ptr p, f32x4 v) { *(__attribute__((address_space(3))) f32x4*)p = v; }
DI s16x4 lds_tr(lds_cptr p) { return __builtin_bit_cast(s16x4, __builtin_amdgcn_ds_read_tr16_b64_v4i16((__attribute__((address_space(3))) v4i16_t*)p)); }
DI bf16x8 cat(s16x4 a, s16x4 b) { return __builtin_shufflevector(a, b, 0, 1, 2, 3, 4, 5, 6, 7); }
DI u32x4 pack8(const f32x16& x, int s, float sc) {
    u32x4 p;
    p[0] = pack_bf16(x[8 * s + 0] * sc, x[8 * s + 1] * sc); p[1] = pack_bf16(x[8 * s + 2] * sc, x[8 * s + 3] * sc);
    p[2] = pack_bf16(x[8 * s + 4] * sc, x[8 * s + 5] * sc); p[3] = pack_bf16(x[8 * s + 6] * sc, x[8 * s + 7] * sc);
    return p;
}
DI u32x4 pack44(f32x4 a, f32x4 b, float sc) { u32x4 p; p[0] = pack_bf16(a[0] * sc, a[1] * sc); p[1] = pack_bf16(a[2] * sc, a[3] * sc); p[2] = pack_bf16(b[0] * sc, b[1] * sc); p[3] = pack_bf16(b[2] * sc, b[3] * sc); return p; }

struct T64 { const char* xs; const char* ws; int xoff[2], woff[2][2]; };
template <int KTOT, bool XFRAG, bool PERMN>
DI T64 t64_setup(const char* xsrc, const char* W, int tok0, int n0, int w, int lane) {
    T64 t;
    if (XFRAG) t.xs = xsrc + ((size_t)((tok0 >> 5) + (w >> 2)) * (KTOT / 16) + (w & 3)) * 1024 + lane * 16;
    else { int row = 8 * w + (lane >> 3); int ch = (lane & 7) ^ ((row >> 1) & 7); t.xs = xsrc + (size_t)(tok0 + row) * 1024 + ch * 16; }
    { int row = 8 * w + (lane >> 3); int ch = (lane & 7) ^ (((row >> 1) & 1) << 2); t.ws = W + (size_t)row * 1024 + n0 * 2 + ch * 16; }
    const int ti = w & 1, ni = (w >> 1) & 1, kh = w >> 2, h = lane >> 5, r32 = lane & 31;
#pragma unroll
    for (int i = 0; i < 2; ++i) {
        const int ks = 2 * kh + i;
        if (XFRAG) t.xoff[i] = (ti * 4 + ks) * 1024 + lane * 16;
        else { int tok = ti * 32 + r32; t.xoff[i] = tok * 128 + (((2 * ks + h) ^ ((tok >> 1) & 7)) * 16); }
        const int g = (lane >> 4) & 1, q = (lane & 15) >> 2, p = lane & 3;
        const int pp = PERMN ? (((p & 1) << 1) | (p >> 1)) : p;
        const int col = ni * 32 + g * 16 + 4 * pp;
#pragma unroll
        for (int tt = 0; tt < 2; ++tt) {
            const int row = XFRAG ? (16 * ks + 8 * tt + 4 * h + q) : (16 * ks + 8 * h + 4 * tt + q);
            t.woff[i][tt] = 8192 + row * 128 + (((col >> 3) ^ (((row >> 1) & 1) << 2)) * 16) + (col & 7) * 2;
        }
    }
    return t;
}
DI void t64_compute(const T64& t, lds_cptr b, f32x16& acc) {
#pragma unroll
    for (int i = 0; i < 2; ++i) {
        bf16x8 xf = lds_b128(b + t.xoff[i]);
        bf16x8 wf = cat(lds_tr(b + t.woff[i][0]), lds_tr(b + t.woff[i][1]));
        acc = MFMA(wf, xf, acc);
    }
}
DI void t64_xchg_write(const f32x16& acc, lds_ptr scr, int w, int lane) {
    const int kh = w >> 2;
    f32x4 a = kh ? (f32x4){acc[0], acc[1], acc[2], acc[3]} : (f32x4){acc[8], acc[9], acc[10], acc[11]};
    f32x4 b = kh ? (f32x4){acc[4], acc[5], acc[6], acc[7]} : (f32x4){acc[12], acc[13], acc[14], acc[15]};
    lds_st_f4(scr + w * 2048 + lane * 16, a); lds_st_f4(scr + w * 2048 + 1024 + lane * 16, b);
}
DI void t64_xchg_read(const f32x16& acc, lds_cptr scr, int w, int lane, f32x4& r0, f32x4& r1) {
    const int kh = w >> 2;
    r0 = kh ? (f32x4){acc[8], acc[9], acc[10], acc[11]} : (f32x4){acc[0], acc[1], acc[2], acc[3]};
    r1 = kh ? (f32x4){acc[12], acc[13], acc[14], acc[15]} : (f32x4){acc[4], acc[5], acc[6], acc[7]};
    r0 += lds_f4(scr + (w ^ 4) * 2048 + lane * 16); r1 += lds_f4(scr + (w ^ 4) * 2048 + 1024 + lane * 16);
}

__global__ __launch_bounds__(512, 2) void k_qkv(const char* __restrict__ x, const char* __restrict__ wq, const char* __restrict__ wk, const char* __restrict__ wv,
                                                const char* __restrict__ gw, char* __restrict__ ws) {
    extern __shared__ __attribute__((aligned(16))) char lds[];
    const int tid = threadIdx.x, lane = tid & 63; const int w = __builtin_amdgcn_readfirstlane(tid >> 6);
    const int b = blockIdx.x, tt64 = (b & 7) * 4 + (b >> 6), cg = (b >> 3) & 7;
    STAMP(0, 0);
    const unsigned lds0 = (unsigned)(uintptr_t)lds;
    const int gtok = tt64 * 64 + cg * 8 + w;
    u32x4 gx = gld16_async(x + (size_t)gtok * 1024 + lane * 16), gg = gld16_async(gw + lane * 16);
    const bool kv = cg < 2;
    if (kv) {
        const char* src = ((lane & 2) ? wk : wv) + (size_t)(64 * w + (lane >> 2)) * 32 + (lane & 1) * 16;
#pragma unroll
        for (int i = 0; i < 4; ++i) glds16(src + i * 512, lds0 + 131072 + w * 4096 + i * 1024);
    }
    const T64 t = t64_setup<512, false, false>(x, wq, tt64 * 64, cg * 64, w, lane);
#pragma unroll
    for (int s = 0; s < 8; ++s) { glds16(t.xs + s * 128, lds0 + s * 16384 + w * 1024); glds16(t.ws + (size_t)s * 65536, lds0 + s * 16384 + 8192 + w * 1024); }
    unsigned long long ta = 0, tb = 0;
    if (b == 0 && tid < 16) __hip_atomic_store((gu32*)(ws + WS_CNT + tid * 256), 0u, RLX_AGENT);
    asm volatile("s_waitcnt vmcnt(16)" : "+v"(gx), "+v"(gg) :: "memory");
    {
        float s = 0.f;
#pragma unroll
        for (int i = 0; i < 4; ++i) s += bf_lo(gx[i]) * bf_lo(gg[i]) + bf_hi(gx[i]) * bf_hi(gg[i]);
#pragma unroll
        for (int o = 32; o >= 1; o >>= 1) s += __shfl_xor(s, o);
        gx[0] = __float_as_uint(1.0f / (1.0f + __expf(-s)));
    }
    f32x16 acc = {}, aT = {}, aN = {};
    const int h = lane >> 5, r32 = lane & 31;
    const int kvx = (cg * 32 + r32) * 128, kvsw = ((cg * 32 + r32) >> 1) & 7;
    const int kvw = 131072 + w * 4096 + (8 * h + ((lane & 15) >> 2)) * 64 + ((lane >> 4) & 1) * 32 + (lane & 3) * 8;
#define QS(s) do { wait_vm_bar<2 * (7 - (s))>(); t64_compute(t, (lds_cptr)lds + (s) * 16384, acc); \
        if (kv && w == (s)) { _Pragma("unroll") for (int ks = 0; ks < 4; ++ks) { \
            bf16x8 xf = lds_b128((lds_cptr)lds + (s) * 16384 + kvx + (((2 * ks + h) ^ kvsw) * 16)); \
            bf16x8 wf = cat(lds_tr((lds_cptr)lds + kvw + ks * 1024), lds_tr((lds_cptr)lds + kvw + ks * 1024 + 256)); \
            aT = MFMA(wf, xf, aT); aN = MFMA(xf, wf, aN); } } } while (0)
    QS(0); STAMP(0, 1); QS(1); QS(2); QS(3); QS(4); QS(5); QS(6); QS(7);
#undef QS
    STAMP(0, 2);
    if (lane == 0) ((float*)(ws + WS_G))[gtok] = __uint_as_float(gx[0]);
    t64_xchg_write(acc, (lds_ptr)lds, w, lane);
    if (kv) {
        lds_ptr p = (lds_ptr)lds + 16384 + w * 6144 + lane * 16;
        lds_st_f4(p, (f32x4){aT[8], aT[9], aT[10], aT[11]}); lds_st_f4(p + 1024, (f32x4){aT[12], aT[13], aT[14], aT[15]});
#pragma unroll
        for (int i = 0; i < 4; ++i) lds_st_f4(p + 2048 + i * 1024, (f32x4){aN[4 * i], aN[4 * i + 1], aN[4 * i + 2], aN[4 * i + 3]});
    }
    lds_bar();
    f32x4 r0, r1; t64_xchg_read(acc, (lds_cptr)lds, w, lane, r0, r1);
    *(u32x4*)(ws + WS_QF + ((size_t)(tt64 * 2 + (w & 1)) * 32 + cg * 4 + ((w >> 1) & 1) * 2 + (w >> 2)) * 1024 + lane * 16) = pack44(r0, r1, QSCALE);
    if (kv && w < 3) {
        f32x4 s0 = {}, s1 = {};
#pragma unroll
        for (int i = 0; i < 8; ++i) { lds_cptr p = (lds_cptr)lds + 16384 + i * 6144 + w * 2048 + lane * 16; s0 += lds_f4(p); s1 += lds_f4(p + 1024); }
        u32x4 v = pack44(s0, s1, 1.0f);
        if (w > 0 && r32 >= 16) { unsigned c = (r32 == 16 || r32 == 20) ? 0x3F803F80u : 0u; v = (u32x4){c, c, c, c}; }
        *(u32x4*)(ws + WS_KV + (size_t)(tt64 * 2 + cg) * 3072 + w * 1024 + lane * 16) = v;
    }
    STAMP(0, 3);
}

DI void up_job(int rp, int cp, const char* x, const char* wg, const char* wu, char* ws, char* lds, int w, int lane) {
    constexpr int NS = 3, STB = 24576, NST = 16;
    const unsigned lds0 = (unsigned)(uintptr_t)lds;
    const char* xs; { int row = 16 * w + (lane >> 2); int ch = (lane & 3) ^ ((row >> 2) & 3); xs = x + (size_t)(rp * 128 + row) * 1024 + ch * 16; }
    const char *gs, *us; { int row = 4 * w + (lane >> 4); int ch = (lane & 15) ^ ((row & 3) << 2); size_t o = (size_t)row * (F_ * 2) + cp * 256 + ch * 16; gs = wg + o; us = wu + o; }
    auto stage = [&](int s, int slot) {
        const unsigned b = lds0 + slot * STB + w * 1024;
        glds16(xs + s * 64, b);
        glds16(gs + (size_t)s * 32 * F_ * 2, b + 8192);
        glds16(us + (size_t)s * 32 * F_ * 2, b + 16384);
    };
    const int th = w & 1, fg = w >> 1, h = lane >> 5, r32 = lane & 31;
    int xoff[2][2], woff[2][2];
#pragma unroll
    for (int ks = 0; ks < 2; ++ks) {
#pragma unroll
        for (int i = 0; i < 2; ++i) { int tok = th * 64 + i * 32 + r32; xoff[ks][i] = tok * 64 + (((2 * ks + h) ^ ((tok >> 2) & 3)) * 16); }
        const int g = (lane >> 4) & 1, q = (lane & 15) >> 2, p = lane & 3;
        const int col = fg * 32 + g * 16 + 4 * p;
#pragma unroll
        for (int t = 0; t < 2; ++t) { int row = 16 * ks + 8 * h + 4 * t + q; woff[ks][t] = 8192 + row * 256 + (((col >> 3) ^ ((row & 3) << 2)) * 16) + (col & 7) * 2; }
    }
    f32x16 ag[2] = {}, au[2] = {};
    stage(0, 0); stage(1, 1);
    int slot = 0;
#pragma unroll 1
    for (int s = 0; s < NST; ++s) {
        if (s + 1 < NST) wait_vm_bar<3>(); else wait_vm_bar<0>();
        if (s + 2 < NST) stage(s + 2, slot == 0 ? 2 : slot - 1);
        const lds_cptr b = (lds_cptr)lds + slot * STB;
#pragma unroll
        for (int ks = 0; ks < 2; ++ks) {
            bf16x8 x0 = lds_b128(b + xoff[ks][0]), x1 = lds_b128(b + xoff[ks][1]);
            bf16x8 gf = cat(lds_tr(b + woff[ks][0]), lds_tr(b + woff[ks][1]));
            bf16x8 uf = cat(lds_tr(b + 8192 + woff[ks][0]), lds_tr(b + 8192 + woff[ks][1]));
            ag[0] = MFMA(gf, x0, ag[0]); ag[1] = MFMA(gf, x1, ag[1]);
            au[0] = MFMA(uf, x0, au[0]); au[1] = MFMA(uf, x1, au[1]);
        }
        slot = slot == 2 ? 0 : slot + 1;
    }
#pragma unroll
    for (int i = 0; i < 2; ++i) {
        f32x16 hh = ag[i] * au[i];
        char* dst = ws + WS_HF + ((size_t)(rp * 4 + th * 2 + i) * 112 + cp * 8 + fg * 2) * 1024 + lane * 16;
        gst16_wt(dst, pack8(hh, 0, 1.0f));
        gst16_wt(dst + 1024, pack8(hh, 1, 1.0f));
    }
    wait_vm_bar<0>();
    if (threadIdx.x == 0) __hip_atomic_fetch_add((gu32*)(ws + WS_CNT + rp * 256), 1u, RLX_AGENT);
}

DI void down_job(int tp, int cg, const char* wd, char* ws, char* lds, int w, int lane) {
    constexpr int NS = 5, NST = 28;
    const unsigned lds0 = (unsigned)(uintptr_t)lds;
    const T64 t = t64_setup<1792, true, true>(ws + WS_HF, wd, tp * 64, cg * 64, w, lane);
#pragma unroll
    for (int s = 0; s < NS - 1; ++s) glds16(t.ws + (size_t)s * 65536, lds0 + s * 16384 + 8192 + w * 1024);
    if (w == 0) {
        gu32* cnt = (gu32*)(ws + WS_CNT + (tp >> 1) * 256);
        for (unsigned spins = 0; spins < (1u << 22); ++spins) {
            if ((unsigned)__builtin_amdgcn_readfirstlane(__hip_atomic_load(cnt, RLX_AGENT)) >= 14u) break;
            __builtin_amdgcn_s_sleep(4);
        }
        __builtin_amdgcn_fence(__ATOMIC_ACQUIRE, "agent");
    }
    wait_vm_bar<0>();
#pragma unroll
    for (int s = 0; s < NS - 1; ++s) glds16(t.xs + (size_t)s * 4096, lds0 + s * 16384 + w * 1024);
    f32x16 acc = {};
    int slot = 0;
#pragma unroll 1
    for (int s = 0; s < NST; ++s) {
        if (s == 0) wait_vm_bar<3>(); else if (s == 1) wait_vm_bar<4>(); else if (s == 2) wait_vm_bar<5>(); else if (s <= NST - 4) wait_vm_bar<6>();
        else if (s == NST - 3) wait_vm_bar<4>(); else if (s == NST - 2) wait_vm_bar<2>(); else wait_vm_bar<0>();
        if (s + NS - 1 < NST) { const int sl = slot == 0 ? NS - 1 : slot - 1; const unsigned b = lds0 + sl * 16384 + w * 1024;
            glds16(t.xs + (size_t)(s + NS - 1) * 4096, b); glds16(t.ws + (size_t)(s + NS - 1) * 65536, b + 8192); }
        t64_compute(t, (lds_cptr)lds + slot * 16384, acc);
        slot = slot == NS - 1 ? 0 : slot + 1;
    }
    lds_bar();
    t64_xchg_write(acc, (lds_ptr)lds, w, lane);
    lds_bar();
    f32x4 r0, r1; t64_xchg_read(acc, (lds_cptr)lds, w, lane, r0, r1);
    *(u32x4*)(ws + WS_BB + ((size_t)(tp * 8 + cg) * 8 + w) * 1024 + lane * 16) = pack44(r0, r1, 1.0f);
}

constexpr int KVT = 3072, CHT = 8, CHB = CHT * KVT;
DI float xhalf_max(float m) { auto rr = __builtin_amdgcn_permlane32_swap(__float_as_uint(m), __float_as_uint(m), false, false); return fmaxf(__uint_as_float(rr[0]), __uint_as_float(rr[1])); }
DI float max16(const f32x16& s) {
    float a = fmaxf(fmaxf(s[0], s[1]), s[2]), b = fmaxf(fmaxf(s[3], s[4]), s[5]);
    a = fmaxf(fmaxf(a, s[6]), s[7]); b = fmaxf(fmaxf(b, s[8]), s[9]); a = fmaxf(fmaxf(a, s[10]), s[11]); b = fmaxf(fmaxf(b, s[12]), s[13]);
    a = fmaxf(fmaxf(a, s[14]), s[15]); return fmaxf(a, b);
}
DI u32x4 exp_pack(const f32x16& s, int o) {
    u32x4 p;
#pragma unroll
    for (int i = 0; i < 4; ++i) p[i] = pack_bf16(__builtin_amdgcn_exp2f(s[o + 2 * i]), __builtin_amdgcn_exp2f(s[o + 2 * i + 1]));
    return p;
}
DI f32x16 attn_exact(const char* kv, bf16x8 qf, int lane) {
    f32x16 o = {}; float m = -INFINITY;
#pragma unroll 1
    for (int t = 0; t < 64; ++t) {
        const char* p = kv + (size_t)t * KVT + lane * 16;
        bf16x8 kf = *(const bf16x8*)p, v0 = *(const bf16x8*)(p + 1024), v1 = *(const bf16x8*)(p + 2048);
        f32x16 s = MFMA(kf, qf, (f32x16){});
        const float mn = fmaxf(m, xhalf_max(max16(s)));
        const float al = __builtin_amdgcn_exp2f(m - mn);
        m = mn;
#pragma unroll
        for (int i = 0; i < 16; ++i) { o[i] *= al; s[i] -= mn; }
        o = MFMA(v0, __builtin_bit_cast(bf16x8, exp_pack(s, 0)), o);
        o = MFMA(v1, __builtin_bit_cast(bf16x8, exp_pack(s, 8)), o);
    }
    return o;
}
#define A_E(r) "v_exp_f32 v" #r ", v" #r "\n\t"
#define A_C(d, a, b) "v_cvt_pk_bf16_f32 v" #d ", v" #a ", v" #b "\n\t"
#define A_QK(d) "v_mfma_f32_32x32x16_bf16 v[" d "], v[100:103], v[96:99], v[80:95]\n\t"
#define A_PV(a, p) "v_mfma_f32_32x32x16_bf16 v[32:47], v[" a "], v[" p "], v[32:47]\n\t"
#define A_W "s_waitcnt lgkmcnt(2)\n\t"
#define A_RD(d, base, off) "ds_read_b128 v[" d "], " base " offset:" #off "\n\t"
#define A_T(S0,S1,S2,S3,S4,S5,S6,S7,S8,S9,S10,S11,S12,S13,S14,S15,P14,P15,SN, BV1,OV1, BK,OK, BV0,OV0) \
    A_E(S0) A_C(119,P14,P15) A_E(S1) A_E(S2) A_W A_PV("108:111","116:119") A_RD("108:111", BV1, OV1) A_E(S3) A_C(112,S0,S1) A_E(S4) A_E(S5) A_C(113,S2,S3) \
    A_W A_QK(SN) A_RD("100:103", BK, OK) A_E(S6) A_E(S7) A_C(114,S4,S5) A_E(S8) A_E(S9) A_C(115,S6,S7) A_E(S10) A_E(S11) \
    A_W A_PV("104:107","112:115") A_RD("104:107", BV0, OV0) A_C(116,S8,S9) A_E(S12) A_E(S13) A_C(117,S10,S11) A_E(S14) A_E(S15) A_C(118,S12,S13)
#define A_TA(BV1,OV1,BK,OK,BV0,OV0) A_T(48,49,50,51,52,53,54,55,56,57,58,59,60,61,62,63,78,79,"64:79",BV1,OV1,BK,OK,BV0,OV0)
#define A_TB(BV1,OV1,BK,OK,BV0,OV0) A_T(64,65,66,67,68,69,70,71,72,73,74,75,76,77,78,79,62,63,"48:63",BV1,OV1,BK,OK,BV0,OV0)
#define A_OPS "+{v[32:47]}"(o), "+{v[48:63]}"(sA), "+{v[64:79]}"(sB), "+{v[100:103]}"(kn), "+{v[104:107]}"(v0), "+{v[108:111]}"(v1), "+{v[112:115]}"(p0), "+{v[116:119]}"(p1)
DI void attn_job(int blk, char* ws, char* lds, int w, int lane) {
    const int tt = (blk & 7) * 8 + (blk >> 5), head = ((blk >> 3) & 3) * 8 + w;
    const unsigned lds0 = (unsigned)(uintptr_t)lds;
    const char* kvs = ws + WS_KV + w * KVT + lane * 16;
    auto stage = [&](int c, int slot) {
        const unsigned b = lds0 + slot * CHB + w * KVT;
        const char* s = kvs + (size_t)c * CHB;
        glds16(s, b); glds16(s + 1024, b + 1024); glds16(s + 2048, b + 2048);
    };
    u32x4 qraw = gld16_async(ws + WS_QF + ((size_t)tt * 32 + head) * 1024 + lane * 16);
    stage(0, 0); stage(1, 1);
    asm volatile("s_waitcnt vmcnt(3)\n\ts_barrier" : "+v"(qraw) :: "memory");
    const bf16x8 qf = __builtin_bit_cast(bf16x8, qraw);
    const lds_cptr lb = (lds_cptr)lds + lane * 16;
    f32x16 cn;
    { f32x16 s0 = MFMA(lds_b128(lb), qf, (f32x16){}); const float m = xhalf_max(max16(s0));
#pragma unroll
      for (int i = 0; i < 16; ++i) cn[i] = -m; }
    f32x16 o = {};
    f32x16 sA = MFMA(lds_b128(lb), qf, cn), sB = {};
    bf16x8 kn = lds_b128(lb + KVT);
    bf16x8 v0 = lds_b128(lb + 1024), v1 = v0;
    u32x4 p0 = {0u, 0u, 0u, 0u}, p1 = {0u, 0u, 0u, 0u};
    asm volatile("s_nop 15" : A_OPS);
    unsigned b = lds0 + lane * 16; int slot = 0;
#pragma unroll 1
    for (int c = 0; c < 8; ++c) {
        const int sn = slot == 2 ? 0 : slot + 1;
        const unsigned bn = lds0 + lane * 16 + sn * CHB;
        asm volatile(A_TA("%8", 2048, "%8", 6144, "%8", 4096) A_TB("%8", 5120, "%8", 9216, "%8", 7168)
                     A_TA("%8", 8192, "%8", 12288, "%8", 10240) A_TB("%8", 11264, "%8", 15360, "%8", 13312)
                     : A_OPS : "v"(b), "{v[80:95]}"(cn), "{v[96:99]}"(qf) : "memory");
        asm volatile("s_waitcnt vmcnt(0)\n\ts_barrier" ::: "memory");
        if (c + 2 < 8) stage(c + 2, slot == 0 ? 2 : slot - 1);
        asm volatile(A_TA("%8", 14336, "%8", 18432, "%8", 16384) A_TB("%8", 17408, "%8", 21504, "%8", 19456)
                     A_TA("%8", 20480, "%9", 0, "%8", 22528) A_TB("%8", 23552, "%9", 3072, "%9", 1024)
                     : A_OPS : "v"(b), "v"(bn), "{v[80:95]}"(cn), "{v[96:99]}"(qf) : "memory");
        b = bn; slot = sn;
    }
    asm volatile("s_nop 0\n\t" A_C(119,78,79) "s_waitcnt lgkmcnt(0)\n\ts_nop 1\n\t" A_PV("108:111","116:119") "s_nop 15" : A_OPS :: "memory");
    float mx = fabsf(o[0]);
#pragma unroll
    for (int i = 1; i < 8; ++i) mx = fmaxf(mx, fabsf(o[i]));
    const bool ok = (o[8] < 1.0e18f) && (o[8] > 0.5f) && (mx < 1.0e36f);
    if (__builtin_expect(!__all(ok), 0)) o = attn_exact(ws + WS_KV, qf, lane);
    const float inv = 1.0f / o[8];
    *(u32x4*)(ws + WS_OF + ((size_t)tt * 32 + head) * 1024 + lane * 16) = pack8(o, 0, inv);
}

#define ROLEMASK 3
__global__ __launch_bounds__(512, 4) void k_main(const char* __restrict__ x, const char* __restrict__ wg, const char* __restrict__ wu, const char* __restrict__ wd, char* __restrict__ ws) {
    extern __shared__ __attribute__((aligned(16))) char lds[];
    const int tid = threadIdx.x, lane = tid & 63; const int w = __builtin_amdgcn_readfirstlane(tid >> 6);
    const int b = blockIdx.x;
    STAMP(1, 0);
    if (b < 256) {
        if (ROLEMASK & 1) {
        attn_job(b, ws, lds, w, lane); }
    } else if (ROLEMASK & 2) {
        const int g = b - 256, xcd = g & 7, i = g >> 3;
        if (i < 28) up_job(4 * (xcd >> 1) + i / 7, 7 * (xcd & 1) + i % 7, x, wg, wu, ws, lds, w, lane);
        STAMP(1, 1);
        down_job(4 * xcd + (i >> 3), i & 7, wd, ws, lds, w, lane);
    }
    STAMP(1, 3);
}

__global__ __launch_bounds__(512, 2) void k_out(const char* __restrict__ wo, char* __restrict__ ws, char* __restrict__ out) {
    extern __shared__ __attribute__((aligned(16))) char lds[];
    const int tid = threadIdx.x, lane = tid & 63; const int w = __builtin_amdgcn_readfirstlane(tid >> 6);
    const int b = blockIdx.x, tp = (b & 7) * 4 + (b >> 6), cg = (b >> 3) & 7;
    STAMP(2, 0);
    const unsigned lds0 = (unsigned)(uintptr_t)lds;
    const T64 t = t64_setup<512, true, true>(ws + WS_OF, wo, tp * 64, cg * 64, w, lane);
#pragma unroll
    for (int s = 0; s < 8; ++s) { glds16(t.xs + s * 4096, lds0 + s * 16384 + w * 1024); glds16(t.ws + (size_t)s * 65536, lds0 + s * 16384 + 8192 + w * 1024); }
    const int tok = tp * 64 + (w & 1) * 32 + (lane & 31);
    u32x4 bv = gld16_async(ws + WS_BB + ((size_t)(tp * 8 + cg) * 8 + w) * 1024 + lane * 16);
    unsigned gu = gld4_async(ws + WS_G + tok * 4);
    f32x16 acc = {};
#define OS(s) do { wait_vm_bar<2 * (7 - (s)) + 2>(); t64_compute(t, (lds_cptr)lds + (s) * 16384, acc); } while (0)
    OS(0); STAMP(2, 1); OS(1); OS(2); OS(3); OS(4); OS(5); OS(6); OS(7);
#undef OS
    STAMP(2, 2);
    t64_xchg_write(acc, (lds_ptr)lds, w, lane);
    asm volatile("s_waitcnt vmcnt(0) lgkmcnt(0)\n\ts_barrier" : "+v"(bv), "+v"(gu) :: "memory");
    f32x4 r0, r1; t64_xchg_read(acc, (lds_cptr)lds, w, lane, r0, r1);
    const float g = __uint_as_float(gu), g1 = 1.0f - g;
    u32x4 r;
    r[0] = pack_bf16(g * r0[0] + g1 * bf_lo(bv[0]), g * r0[1] + g1 * bf_hi(bv[0])); r[1] = pack_bf16(g * r0[2] + g1 * bf_lo(bv[1]), g * r0[3] + g1 * bf_hi(bv[1]));
    r[2] = pack_bf16(g * r1[0] + g1 * bf_lo(bv[2]), g * r1[1] + g1 * bf_hi(bv[2])); r[3] = pack_bf16(g * r1[2] + g1 * bf_lo(bv[3]), g * r1[3] + g1 * bf_hi(bv[3]));
    *(u32x4*)(out + (size_t)tok * 1024 + (cg * 64 + ((w >> 1) & 1) * 32 + (w >> 2) * 16 + 8 * (lane >> 5)) * 2) = r;
    STAMP(2, 3);
}

extern "C" void kernel_launch(void* const* d_in, const int* in_sizes, int n_in, void* d_out, int out_size, void* d_ws, size_t ws_size, hipStream_t stream) {
    const char* x = (const char*)d_in[0]; const char* wq = (const char*)d_in[1]; const char* wk = (const char*)d_in[2]; const char* wv = (const char*)d_in[3];
    const char* wo = (const char*)d_in[4]; const char* wg = (const char*)d_in[6]; const char* wu = (const char*)d_in[7]; const char* wd = (const char*)d_in[8];
    const char* gw = (const char*)d_in[9];
    char* ws = (char*)d_ws;
    (void)hipFuncSetAttribute((const void*)k_qkv, hipFuncAttributeMaxDynamicSharedMemorySize, 163840);
    (void)hipFuncSetAttribute((const void*)k_main, hipFuncAttributeMaxDynamicSharedMemorySize, 81920);
    (void)hipFuncSetAttribute((const void*)k_out, hipFuncAttributeMaxDynamicSharedMemorySize, 131072);
    k_qkv<<<256, 512, 163840, stream>>>(x, wq, wk, wv, gw, ws);
    k_main<<<512, 512, 81920, stream>>>(x, wg, wu, wd, ws);
    k_out<<<256, 512, 131072, stream>>>(wo, ws, (char*)d_out);
}
```

```cpp
#include <hip/hip_runtime.h>
#include <hip/hip_bf16.h>
#include <stdint.h>

typedef __attribute__((ext_vector_type(8))) short bf16x8;
typedef __attribute__((ext_vector_type(4))) short s16x4;
typedef __attribute__((ext_vector_type(16))) float f32x16;
typedef __attribute__((ext_vector_type(4))) float f32x4;
typedef __attribute__((ext_vector_type(4))) unsigned u32x4;
typedef __attribute__((ext_vector_type(2))) float f32x2;
typedef __attribute__((ext_vector_type(2))) __bf16 bf16x2_t;
typedef __attribute__((address_space(3))) const char* lds_cptr;
typedef __attribute__((address_space(3))) char* lds_ptr;
typedef __attribute__((address_space(1))) unsigned gu32;
typedef __attribute__((address_space(1))) unsigned long long gu64;
typedef short v4i16_t __attribute__((ext_vector_type(4)));
#define DI __device__ __forceinline__
#define MFMA(a, b, c) __builtin_amdgcn_mfma_f32_32x32x16_bf16((a), (b), (c), 0, 0, 0)
#define RLX_AGENT __ATOMIC_RELAXED, __HIP_MEMORY_SCOPE_AGENT
#define RFL(x) __builtin_amdgcn_readfirstlane(x)

#define KVSLEEP 6
#define CNTSLEEP 10
constexpr int F_ = 1792;
constexpr float QSCALE = 0.25f * 1.4426950408889634f;
constexpr unsigned SPIN_CAP = 1u << 20;

constexpr unsigned WS_CNT = 0;
constexpr unsigned WS_ACK = 6144;
constexpr unsigned WS_REQ = 16384;
constexpr unsigned WS_ECHO = 20480;
constexpr unsigned WS_G  = 24576;
constexpr unsigned WS_KV = 32768;
constexpr unsigned WS_OF = 262144;
constexpr unsigned WS_HF = WS_OF + 2097152;
constexpr unsigned WS_DBG = 16u * 1024 * 1024;
constexpr int CNT_H = 0, CNT_O = 16;
constexpr unsigned WS_KVF = 8192;

#define STAMP(i)

DI unsigned pack_bf16(float lo, float hi) { f32x2 v = {lo, hi}; bf16x2_t r = __builtin_convertvector(v, bf16x2_t); return __builtin_bit_cast(unsigned, r); }
DI void glds(const char* sbase, unsigned voff, unsigned lds_base) {
    unsigned sv;
    asm volatile("s_mov_b32 %0, m0\n\ts_mov_b32 m0, %3\n\ts_nop 0\n\tglobal_load_lds_dwordx4 %1, %2\n\ts_mov_b32 m0, %0"
                 : "=&s"(sv) : "v"(voff), "s"(sbase), "s"(RFL(lds_base)) : "memory");
}
DI void glds_dev(const char* sbase, unsigned voff, unsigned lds_base) {
    unsigned sv;
    asm volatile("s_mov_b32 %0, m0\n\ts_mov_b32 m0, %3\n\ts_nop 0\n\tglobal_load_lds_dwordx4 %1, %2 sc1\n\ts_mov_b32 m0, %0"
                 : "=&s"(sv) : "v"(voff), "s"(sbase), "s"(RFL(lds_base)) : "memory");
}
DI u32x4 gld16_async(const char* sbase, unsigned voff) { u32x4 r; asm volatile("global_load_dwordx4 %0, %1, %2" : "=v"(r) : "v"(voff), "s"(sbase) : "memory"); return r; }
DI unsigned gld4_async(const char* sbase, unsigned voff) { unsigned r; asm volatile("global_load_dword %0, %1, %2" : "=v"(r) : "v"(voff), "s"(sbase) : "memory"); return r; }
DI void gst16_dev(char* sbase, unsigned voff, u32x4 v) { asm volatile("global_store_dwordx4 %0, %1, %2 sc1\n\ts_nop 1" :: "v"(voff), "v"(v), "s"(sbase) : "memory"); }
template <int N> DI void wait_vm_bar() { asm volatile("s_waitcnt vmcnt(%0) lgkmcnt(0)\n\ts_barrier" :: "i"(N) : "memory"); }
DI void lds_bar() { asm volatile("s_waitcnt lgkmcnt(0)\n\ts_barrier" ::: "memory"); }
DI bf16x8 lds_b128(lds_cptr p) { return *(const __attribute__((address_space(3))) bf16x8*)p; }
DI f32x4 lds_f4(lds_cptr p) { return *(const __attribute__((address_space(3))) f32x4*)p; }
DI void lds_st_f4(lds_ptr p, f32x4 v) { *(__attribute__((address_space(3))) f32x4*)p = v; }
DI void lds_st_f(lds_ptr p, float v) { *(__attribute__((address_space(3))) float*)p = v; }
DI float lds_f(lds_cptr p) { return *(const __attribute__((address_space(3))) float*)p; }
DI s16x4 lds_tr(lds_cptr p) { return __builtin_bit_cast(s16x4, __builtin_amdgcn_ds_read_tr16_b64_v4i16((__attribute__((address_space(3))) v4i16_t*)p)); }
DI bf16x8 cat(s16x4 a, s16x4 b) { return __builtin_shufflevector(a, b, 0, 1, 2, 3, 4, 5, 6, 7); }
DI u32x4 pack8(const f32x16& x, int s, float sc) {
    u32x4 p;
    p[0] = pack_bf16(x[8 * s + 0] * sc, x[8 * s + 1] * sc); p[1] = pack_bf16(x[8 * s + 2] * sc, x[8 * s + 3] * sc);
    p[2] = pack_bf16(x[8 * s + 4] * sc, x[8 * s + 5] * sc); p[3] = pack_bf16(x[8 * s + 6] * sc, x[8 * s + 7] * sc);
    return p;
}
DI u32x4 pack44(f32x4 a, f32x4 b, float sc) { u32x4 p; p[0] = pack_bf16(a[0] * sc, a[1] * sc); p[1] = pack_bf16(a[2] * sc, a[3] * sc); p[2] = pack_bf16(b[0] * sc, b[1] * sc); p[3] = pack_bf16(b[2] * sc, b[3] * sc); return p; }

DI unsigned long long hs_post(char* ws, int lane) {
    const unsigned long long ch = __builtin_amdgcn_s_memrealtime() * 2 + 1;
    if (lane == 0) __hip_atomic_store((gu64*)(ws + WS_REQ) + blockIdx.x, ch, RLX_AGENT);
    return ch;
}
DI void hs_wait(char* ws, unsigned long long ch, int lane) {
    for (unsigned s = 0; s < SPIN_CAP; ++s) {
        const unsigned long long e = __hip_atomic_load((gu64*)(ws + WS_ECHO) + blockIdx.x, RLX_AGENT);
        if ((unsigned)RFL((unsigned)e) == (unsigned)ch && (unsigned)RFL((unsigned)(e >> 32)) == (unsigned)(ch >> 32)) break;
        __builtin_amdgcn_s_sleep(4);
    }
    if (lane == 0) __hip_atomic_store((gu32*)(ws + WS_ACK) + blockIdx.x, 1u, RLX_AGENT);
}
DI void cnt_wait(char* ws, int idx, unsigned need) {
    for (unsigned s = 0; s < SPIN_CAP; ++s) {
        if ((unsigned)RFL(__hip_atomic_load((gu32*)(ws + WS_CNT + idx * 128), RLX_AGENT)) >= need) break;
        __builtin_amdgcn_s_sleep(CNTSLEEP);
    }
}
DI void cnt_add(char* ws, int idx) { __hip_atomic_fetch_add((gu32*)(ws + WS_CNT + idx * 128), 1u, RLX_AGENT); }
DI void hs_leader(char* ws, int lane) {
    if (lane < 48) __hip_atomic_store((gu32*)(ws + WS_CNT + lane * 128), 0u, RLX_AGENT);
#pragma unroll
    for (int i = 0; i < 10; ++i) gst16_dev(ws + WS_ACK + i * 1024, lane * 16, (u32x4){0u, 0u, 0u, 0u});
    asm volatile("s_waitcnt vmcnt(0)" ::: "memory");
    unsigned long long prev[8];
#pragma unroll
    for (int i = 0; i < 8; ++i) prev[i] = 0ull;
    const unsigned long long t0 = __builtin_amdgcn_s_memrealtime() * 2;
    for (unsigned s = 0; s < SPIN_CAP; ++s) {
        unsigned long long r[8];
#pragma unroll
        for (int i = 0; i < 8; ++i) r[i] = __hip_atomic_load((gu64*)(ws + WS_REQ) + i * 64 + lane, RLX_AGENT);
        bool recent = true;
#pragma unroll
        for (int i = 0; i < 8; ++i) {
            if (r[i] != prev[i]) __hip_atomic_store((gu64*)(ws + WS_ECHO) + i * 64 + lane, r[i], RLX_AGENT);
            prev[i] = r[i]; recent &= (r[i] + 400ull - t0) < 4000ull;
        }
        if (__all(recent) || s > 16) {
            unsigned long long a[4];
#pragma unroll
            for (int i = 0; i < 4; ++i) a[i] = __hip_atomic_load((gu64*)(ws + WS_ACK) + i * 64 + lane, RLX_AGENT);
            if (__all((a[0] & a[1] & a[2] & a[3]) == 0x100000001ull)) break;
            __builtin_amdgcn_s_sleep(40);
        }
    }
    __builtin_amdgcn_s_waitcnt(0x0F70);
}

struct T64 { unsigned xs, ws; int xoff[2], woff[2][2]; };
template <int KTOT, bool XFRAG, bool PERMN>
DI T64 t64_setup(int tok0, int n0, int w, int lane) {
    T64 t;
    if (XFRAG) t.xs = (((tok0 >> 5) + (w >> 2)) * (KTOT / 16) + (w & 3)) * 1024 + lane * 16;
    else { int row = 8 * w + (lane >> 3); int ch = (lane & 7) ^ ((row >> 1) & 7); t.xs = (tok0 + row) * 1024 + ch * 16; }
    { int row = 8 * w + (lane >> 3); int ch = (lane & 7) ^ (((row >> 1) & 1) << 2); t.ws = row * 1024 + n0 * 2 + ch * 16; }
    const int ti = w & 1, ni = (w >> 1) & 1, kh = w >> 2, h = lane >> 5, r32 = lane & 31;
#pragma unroll
    for (int i = 0; i < 2; ++i) {
        const int ks = 2 * kh + i;
        if (XFRAG) t.xoff[i] = (ti * 4 + ks) * 1024 + lane * 16;
        else { int tok = ti * 32 + r32; t.xoff[i] = tok * 128 + (((2 * ks + h) ^ ((tok >> 1) & 7)) * 16); }
        const int g = (lane >> 4) & 1, q = (lane & 15) >> 2, p = lane & 3;
        const int pp = PERMN ? (((p & 1) << 1) | (p >> 1)) : p;
        const int col = ni * 32 + g * 16 + 4 * pp;
#pragma unroll
        for (int tt = 0; tt < 2; ++tt) {
            const int row = XFRAG ? (16 * ks + 8 * tt + 4 * h + q) : (16 * ks + 8 * h + 4 * tt + q);
            t.woff[i][tt] = row * 128 + (((col >> 3) ^ (((row >> 1) & 1) << 2)) * 16) + (col & 7) * 2;
        }
    }
    return t;
}
DI void t64_compute(const T64& t, lds_cptr b, f32x16& acc) {
#pragma unroll
    for (int i = 0; i < 2; ++i) {
        bf16x8 xf = lds_b128(b + t.xoff[i]);
        bf16x8 wf = cat(lds_tr(b + 8192 + t.woff[i][0]), lds_tr(b + 8192 + t.woff[i][1]));
        acc = MFMA(wf, xf, acc);
    }
}
DI void t64_xchg_write(const f32x16& acc, lds_ptr scr, int w, int lane) {
    const int kh = w >> 2;
    f32x4 a = kh ? (f32x4){acc[0], acc[1], acc[2], acc[3]} : (f32x4){acc[8], acc[9], acc[10], acc[11]};
    f32x4 b = kh ? (f32x4){acc[4], acc[5], acc[6], acc[7]} : (f32x4){acc[12], acc[13], acc[14], acc[15]};
    lds_st_f4(scr + w * 2048 + lane * 16, a); lds_st_f4(scr + w * 2048 + 1024 + lane * 16, b);
}
DI void t64_xchg_read(const f32x16& acc, lds_cptr scr, int w, int lane, f32x4& r0, f32x4& r1) {
    const int kh = w >> 2;
    r0 = kh ? (f32x4){acc[8], acc[9], acc[10], acc[11]} : (f32x4){acc[0], acc[1], acc[2], acc[3]};
    r1 = kh ? (f32x4){acc[12], acc[13], acc[14], acc[15]} : (f32x4){acc[4], acc[5], acc[6], acc[7]};
    r0 += lds_f4(scr + (w ^ 4) * 2048 + lane * 16); r1 += lds_f4(scr + (w ^ 4) * 2048 + 1024 + lane * 16);
}

DI float bf_lo(unsigned u) { return __uint_as_float(u << 16); }
DI float bf_hi(unsigned u) { return __uint_as_float(u & 0xffff0000u); }
DI void gate_job(int tp, const char* x, const char* gw, char* ws, int w, int lane) {
    const u32x4 gv = *(const u32x4*)(gw + lane * 16);
    float res = 0.f;
#pragma unroll 4
    for (int t = 0; t < 16; ++t) {
        const u32x4 xv = *(const u32x4*)(x + (size_t)(tp * 64 + w * 16 + t) * 1024 + lane * 16);
        float s = 0.f;
#pragma unroll
        for (int i = 0; i < 4; ++i) s += bf_lo(xv[i]) * bf_lo(gv[i]) + bf_hi(xv[i]) * bf_hi(gv[i]);
#pragma unroll
        for (int o = 32; o >= 1; o >>= 1) s += __shfl_xor(s, o);
        if (lane == t) res = s;
    }
    if (lane < 16) __hip_atomic_store((gu32*)(ws + WS_G) + tp * 64 + w * 16 + lane, __float_as_uint(1.0f / (1.0f + __expf(-res))), RLX_AGENT);
    __builtin_amdgcn_s_waitcnt(0x0F70);
}

DI void up_job(int rp, int cp, const char* x, const char* wg, const char* wu, char* ws, char* lds, int w, int lane, unsigned long long ch) {
    constexpr int STB = 24576, NST = 16;
    const unsigned lds0 = (unsigned)(uintptr_t)lds + w * 1024;
    unsigned xs, gs;
    { int row = 16 * w + (lane >> 2); int c = (lane & 3) ^ ((row >> 2) & 3); xs = (rp * 128 + row) * 1024 + c * 16; }
    { int row = 4 * w + (lane >> 4); int c = (lane & 15) ^ ((row & 3) << 2); gs = row * (F_ * 2) + cp * 256 + c * 16; }
#define UP_STAGE(s) do { const unsigned b_ = lds0 + ((s) % 3) * STB; glds(x + (s) * 64, xs, b_); glds(wg + (s) * 32 * F_ * 2, gs, b_ + 8192); glds(wu + (s) * 32 * F_ * 2, gs, b_ + 16384); } while (0)
    UP_STAGE(0); UP_STAGE(1);
    const int th = w & 1, fg = w >> 1, h = lane >> 5, r32 = lane & 31;
    lds_cptr xb[2], wb;
#pragma unroll
    for (int ks = 0; ks < 2; ++ks) { int tok = th * 64 + r32; xb[ks] = (lds_cptr)lds + tok * 64 + (((2 * ks + h) ^ ((tok >> 2) & 3)) * 16); }
    { const int g = (lane >> 4) & 1, q = (lane & 15) >> 2, p = lane & 3; const int col = fg * 32 + g * 16 + 4 * p;
      wb = (lds_cptr)lds + 8192 + (8 * h + q) * 256 + (((col >> 3) ^ (q << 2)) * 16) + (col & 7) * 2; }
    f32x16 ag[2] = {}, au[2] = {};
#pragma unroll
    for (int s = 0; s < NST; ++s) {
        if (s + 1 < NST) wait_vm_bar<3>(); else wait_vm_bar<0>();
        if ((s & 3) == 0) STAMP(8 + (s >> 2));
        if (s + 2 < NST) UP_STAGE(s + 2);
        const int so = (s % 3) * STB;
#pragma unroll
        for (int ks = 0; ks < 2; ++ks) {
            bf16x8 x0 = lds_b128(xb[ks] + so), x1 = lds_b128(xb[ks] + so + 2048);
            bf16x8 gf = cat(lds_tr(wb + so + ks * 4096), lds_tr(wb + so + ks * 4096 + 1024));
            bf16x8 uf = cat(lds_tr(wb + so + 8192 + ks * 4096), lds_tr(wb + so + 8192 + ks * 4096 + 1024));
            ag[0] = MFMA(gf, x0, ag[0]); ag[1] = MFMA(gf, x1, ag[1]);
            au[0] = MFMA(uf, x0, au[0]); au[1] = MFMA(uf, x1, au[1]);
        }
    }
#undef UP_STAGE
    STAMP(2);
#pragma unroll
    for (int i = 0; i < 2; ++i) {
        f32x16 hh = ag[i] * au[i];
        char* dst = ws + WS_HF + ((rp * 4 + th * 2 + i) * 112 + cp * 8 + fg * 2) * 1024;
        gst16_dev(dst, lane * 16, pack8(hh, 0, 1.0f));
        gst16_dev(dst, 1024 + lane * 16, pack8(hh, 1, 1.0f));
    }
    if (w == 0) hs_wait(ws, ch, lane);
    wait_vm_bar<0>();
    if (threadIdx.x == 0) cnt_add(ws, CNT_H + rp);
}

DI void down_out_job(int tp, int cg, int jw, const char* wd, const char* wo, char* ws, char* out, char* lds, int w, int lane) {
    constexpr int NS = 5, NST = 28;
    const unsigned lds0 = (unsigned)(uintptr_t)lds + w * 1024;
    const T64 t = t64_setup<1792, true, true>(tp * 64, cg * 64, w, lane);
    const char* hf = ws + WS_HF;
    const unsigned tch = gld4_async(wd, (jw * 56 + (lane < 56 ? lane : 0)) * 128);
#pragma unroll
    for (int s = 0; s < NS - 1; ++s) glds(wd + s * 65536, t.ws, lds0 + s * 16384 + 8192);
    if (w == 0) cnt_wait(ws, CNT_H + (tp >> 1), 14u);
    wait_vm_bar<0>();
    asm volatile("" :: "v"(tch));
    STAMP(3);
#pragma unroll
    for (int s = 0; s < NS - 1; ++s) glds_dev(hf + s * 4096, t.xs, lds0 + s * 16384);
    f32x16 acc = {};
#pragma unroll
    for (int s = 0; s < NST; ++s) {
        if (s == 0) wait_vm_bar<3>(); else if (s == 1) wait_vm_bar<4>(); else if (s == 2) wait_vm_bar<5>(); else if (s <= NST - 4) wait_vm_bar<6>();
        else if (s == NST - 3) wait_vm_bar<4>(); else if (s == NST - 2) wait_vm_bar<2>(); else wait_vm_bar<0>();
        if (s + NS - 1 < NST) { const unsigned b = lds0 + ((s + NS - 1) % NS) * 16384;
            glds_dev(hf + (s + NS - 1) * 4096, t.xs, b); glds(wd + (s + NS - 1) * 65536, t.ws, b + 8192); }
        t64_compute(t, (lds_cptr)lds + (s % NS) * 16384, acc);
    }
    STAMP(4);
    lds_bar();
#pragma unroll
    for (int s = 0; s < 8; ++s) glds(wo + s * 65536, t.ws, lds0 + s * 8192);
    wait_vm_bar<0>();
    bf16x8 wf[16];
#pragma unroll
    for (int s = 0; s < 8; ++s)
#pragma unroll
        for (int i = 0; i < 2; ++i) wf[2 * s + i] = cat(lds_tr((lds_cptr)lds + s * 8192 + t.woff[i][0]), lds_tr((lds_cptr)lds + s * 8192 + t.woff[i][1]));
    if (w == 0) cnt_wait(ws, CNT_O + tp, 9u);
    lds_bar();
    STAMP(5);
    const unsigned os = ((tp * 2 + (w >> 2)) * 32 + (w & 3)) * 1024 + lane * 16;
#pragma unroll
    for (int s = 0; s < 8; ++s) glds_dev(ws + WS_OF + s * 4096, os, lds0 + s * 8192);
    const int tok = tp * 64 + (w & 1) * 32 + (lane & 31);
    unsigned gu; asm volatile("global_load_dword %0, %1, %2 sc1" : "=v"(gu) : "v"(tok * 4), "s"(ws + WS_G) : "memory");
    f32x16 acca = {};
#pragma unroll
    for (int s = 0; s < 8; ++s) {
        if (s == 0) wait_vm_bar<5>(); else if (s == 4) wait_vm_bar<1>();
#pragma unroll
        for (int i = 0; i < 2; ++i) acca = MFMA(wf[2 * s + i], lds_b128((lds_cptr)lds + s * 8192 + t.xoff[i]), acca);
    }
    asm volatile("s_waitcnt vmcnt(0)" : "+v"(gu) :: "memory");
    const float g = __uint_as_float(gu), g1 = 1.0f - g;
#pragma unroll
    for (int i = 0; i < 16; ++i) acc[i] = g * acca[i] + g1 * acc[i];
    t64_xchg_write(acc, (lds_ptr)lds + 65536, w, lane);
    lds_bar();
    f32x4 r0, r1; t64_xchg_read(acc, (lds_cptr)lds + 65536, w, lane, r0, r1);
    *(u32x4*)(out + (size_t)tok * 1024 + (cg * 64 + ((w >> 1) & 1) * 32 + (w >> 2) * 16 + 8 * (lane >> 5)) * 2) = pack44(r0, r1, 1.0f);
}

constexpr int KVT = 3072, CHT = 8, CHB = CHT * KVT;
DI float xhalf_max(float m) { auto rr = __builtin_amdgcn_permlane32_swap(__float_as_uint(m), __float_as_uint(m), false, false); return fmaxf(__uint_as_float(rr[0]), __uint_as_float(rr[1])); }
DI float max16(const f32x16& s) {
    float a = fmaxf(fmaxf(s[0], s[1]), s[2]), b = fmaxf(fmaxf(s[3], s[4]), s[5]);
    a = fmaxf(fmaxf(a, s[6]), s[7]); b = fmaxf(fmaxf(b, s[8]), s[9]); a = fmaxf(fmaxf(a, s[10]), s[11]); b = fmaxf(fmaxf(b, s[12]), s[13]);
    a = fmaxf(fmaxf(a, s[14]), s[15]); return fmaxf(a, b);
}
DI u32x4 exp_pack(const f32x16& s, int o) {
    u32x4 p;
#pragma unroll
    for (int i = 0; i < 4; ++i) p[i] = pack_bf16(__builtin_amdgcn_exp2f(s[o + 2 * i]), __builtin_amdgcn_exp2f(s[o + 2 * i + 1]));
    return p;
}
DI f32x16 attn_exact(const char* kv, bf16x8 qf, int lane) {
    f32x16 o = {}; float m = -INFINITY;
#pragma unroll 1
    for (int t = 0; t < 64; ++t) {
        u32x4 kf, v0, v1;
        asm volatile("global_load_dwordx4 %0, %3, %4 sc1\n\tglobal_load_dwordx4 %1, %3, %4 offset:1024 sc1\n\tglobal_load_dwordx4 %2, %3, %4 offset:2048 sc1\n\ts_waitcnt vmcnt(0)"
                     : "=&v"(kf), "=&v"(v0), "=&v"(v1) : "v"(t * KVT + lane * 16), "s"(kv) : "memory");
        f32x16 s = MFMA(__builtin_bit_cast(bf16x8, kf), qf, (f32x16){});
        const float mn = fmaxf(m, xhalf_max(max16(s)));
        const float al = __builtin_amdgcn_exp2f(m - mn);
        m = mn;
#pragma unroll
        for (int i = 0; i < 16; ++i) { o[i] *= al; s[i] -= mn; }
        o = MFMA(__builtin_bit_cast(bf16x8, v0), __builtin_bit_cast(bf16x8, exp_pack(s, 0)), o);
        o = MFMA(__builtin_bit_cast(bf16x8, v1), __builtin_bit_cast(bf16x8, exp_pack(s, 8)), o);
    }
    return o;
}
#define A_E(r) "v_exp_f32 v" #r ", v" #r "\n\t"
#define A_C(d, a, b) "v_cvt_pk_bf16_f32 v" #d ", v" #a ", v" #b "\n\t"
#define A_QK(d) "v_mfma_f32_32x32x16_bf16 v[" d "], v[100:103], v[96:99], v[80:95]\n\t"
#define A_PV(a, p) "v_mfma_f32_32x32x16_bf16 v[32:47], v[" a "], v[" p "], v[32:47]\n\t"
#define A_W "s_waitcnt lgkmcnt(2)\n\t"
#define A_RD(d, base, off) "ds_read_b128 v[" d "], " base " offset:" #off "\n\t"
#define A_T(S0,S1,S2,S3,S4,S5,S6,S7,S8,S9,S10,S11,S12,S13,S14,S15,P14,P15,SN, BV1,OV1, BK,OK, BV0,OV0) \
    A_E(S0) A_C(119,P14,P15) A_E(S1) A_E(S2) A_W A_PV("108:111","116:119") A_RD("108:111", BV1, OV1) A_E(S3) A_C(112,S0,S1) A_E(S4) A_E(S5) A_C(113,S2,S3) \
    A_W A_QK(SN) A_RD("100:103", BK, OK) A_E(S6) A_E(S7) A_C(114,S4,S5) A_E(S8) A_E(S9) A_C(115,S6,S7) A_E(S10) A_E(S11) \
    A_W A_PV("104:107","112:115") A_RD("104:107", BV0, OV0) A_C(116,S8,S9) A_E(S12) A_E(S13) A_C(117,S10,S11) A_E(S14) A_E(S15) A_C(118,S12,S13)
#define A_TA(BV1,OV1,BK,OK,BV0,OV0) A_T(48,49,50,51,52,53,54,55,56,57,58,59,60,61,62,63,78,79,"64:79",BV1,OV1,BK,OK,BV0,OV0)
#define A_TB(BV1,OV1,BK,OK,BV0,OV0) A_T(64,65,66,67,68,69,70,71,72,73,74,75,76,77,78,79,62,63,"48:63",BV1,OV1,BK,OK,BV0,OV0)
#define A_OPS "+{v[32:47]}"(o), "+{v[48:63]}"(sA), "+{v[64:79]}"(sB), "+{v[100:103]}"(kn), "+{v[104:107]}"(v0), "+{v[108:111]}"(v1), "+{v[112:115]}"(p0), "+{v[116:119]}"(p1)
DI void attn_role(int blk, const char* x, const char* wq, const char* wk, const char* wv, char* ws, char* lds, int w, int lane, unsigned long long ch) {
    const int xcd = blk & 7, tp = xcd * 4 + (blk >> 6), hg = (blk >> 3) & 7;
    const unsigned lds0 = (unsigned)(uintptr_t)lds;
    const T64 t = t64_setup<512, false, false>(tp * 64, hg * 64, w, lane);
#define Q_STAGE(s) do { const unsigned b_ = lds0 + w * 1024 + (((s) + 4) % 5) * 16384; glds(x + (s) * 128, t.xs, b_); glds(wq + (s) * 65536, t.ws, b_ + 8192); } while (0)
#define Q_COMP(s) t64_compute(t, (lds_cptr)lds + (((s) + 4) % 5) * 16384, acc)
    bool hs_done = false;
    if (hg < 2) {
        const int h = lane >> 5, r32 = lane & 31, tile = tp * 2 + hg;
        const unsigned sb = lds0 + w * 8192;
        { const int row = lane >> 3; const unsigned xo = (tile * 32 + row) * 1024 + w * 128;
#pragma unroll
          for (int i = 0; i < 4; ++i) glds(x + i * 8192, xo + (((lane & 7) ^ ((i * 4 + (row >> 1)) & 7)) * 16), sb + i * 1024);
          const unsigned wo_ = w * 2048 + lane * 16;
          glds(wv, wo_, sb + 4096); glds(wv + 1024, wo_, sb + 5120); glds(wk, wo_, sb + 6144); glds(wk + 1024, wo_, sb + 7168); }
        Q_STAGE(0);
        STAMP(11);
        asm volatile("s_waitcnt vmcnt(2)" ::: "memory");
        STAMP(12);
        f32x16 a = {};
        const lds_cptr xb = (lds_cptr)lds + w * 8192 + r32 * 128;
        const lds_cptr wb = (lds_cptr)lds + w * 8192 + 4096 + ((lane >> 4) & 1) * 2048 + (8 * h + ((lane & 15) >> 2)) * 32 + (lane & 3) * 8;
#pragma unroll
        for (int ks = 0; ks < 4; ++ks) a = MFMA(cat(lds_tr(wb + ks * 512), lds_tr(wb + ks * 512 + 128)), lds_b128(xb + (((2 * ks + h) ^ ((r32 >> 1) & 7)) * 16)), a);
        { lds_ptr p = (lds_ptr)lds + w * 8192;
          lds_st_f4(p + lane * 16, (f32x4){a[8], a[9], a[10], a[11]}); lds_st_f4(p + 1024 + lane * 16, (f32x4){a[12], a[13], a[14], a[15]});
#pragma unroll
          for (int r = 0; r < 8; ++r) lds_st_f(p + 2048 + ((r & 3) + 8 * (r >> 2) + 4 * h) * 144 + r32 * 4, a[r]); }
        lds_bar();
        if (w < 3) {
            f32x4 s0 = {}, s1 = {};
            const lds_cptr p = (lds_cptr)lds + (w == 0 ? lane * 16 : 2048 + (r32 & 15) * 144 + h * 16 + (w - 1) * 64);
            const int d1 = w == 0 ? 1024 : 32;
#pragma unroll
            for (int q = 0; q < 8; ++q) { s0 += lds_f4(p + q * 8192); s1 += lds_f4(p + q * 8192 + d1); }
            u32x4 v = pack44(s0, s1, 1.0f);
            if (w > 0 && r32 >= 16) { unsigned c = (r32 == 16 || r32 == 20) ? 0x3F803F80u : 0u; v = (u32x4){c, c, c, c}; }
            gst16_dev(ws + WS_KV + tile * 3072 + w * 1024, lane * 16, v);
            STAMP(13);
            if (w == 0) hs_wait(ws, ch, lane);
            asm volatile("s_waitcnt vmcnt(0)" ::: "memory");
        }
        hs_done = true;
        lds_bar();
        if (w == 0 && lane < 32) __hip_atomic_store((gu32*)(ws + WS_KVF) + lane * 64 + tile, 1u, RLX_AGENT);
        STAMP(14);
    } else Q_STAGE(0);
    Q_STAGE(1); Q_STAGE(2); Q_STAGE(3); Q_STAGE(4);
    f32x16 acc = {};
    STAMP(9);
    wait_vm_bar<8>(); STAMP(10); Q_COMP(0);
    wait_vm_bar<6>(); Q_STAGE(5); Q_COMP(1);
    wait_vm_bar<6>(); Q_STAGE(6); Q_COMP(2);
    wait_vm_bar<6>(); Q_STAGE(7); Q_COMP(3);
    wait_vm_bar<6>(); Q_COMP(4);
    wait_vm_bar<4>(); Q_COMP(5);
    wait_vm_bar<2>(); Q_COMP(6);
    wait_vm_bar<0>(); Q_COMP(7);
#undef Q_STAGE
#undef Q_COMP
    t64_xchg_write(acc, (lds_ptr)lds + 49152, w, lane);
    lds_bar();
    f32x4 r0, r1; t64_xchg_read(acc, (lds_cptr)lds + 49152, w, lane, r0, r1);
    const bf16x8 qf = __builtin_bit_cast(bf16x8, pack44(r0, r1, QSCALE));
    STAMP(1);
    if (w == 0) {
        if (!hs_done) hs_wait(ws, ch, lane);
        STAMP(8);
        for (unsigned s = 0; s < SPIN_CAP; ++s) {
            if (__all(__hip_atomic_load((gu32*)(ws + WS_KVF) + (blk & 31) * 64 + lane, RLX_AGENT) == 1u)) break;
            __builtin_amdgcn_s_sleep(KVSLEEP);
        }
    }
    lds_bar();
    STAMP(2);
    const char* kv = ws + WS_KV;
    const unsigned kvo = w * KVT + lane * 16;
    auto stage = [&](int c, int slot) {
        const unsigned b = lds0 + slot * CHB + w * KVT; const char* s = kv + c * CHB;
        glds_dev(s, kvo, b); glds_dev(s + 1024, kvo, b + 1024); glds_dev(s + 2048, kvo, b + 2048);
    };
    stage(0, 0); stage(1, 1);
    asm volatile("s_waitcnt vmcnt(3)\n\ts_barrier" ::: "memory");
    STAMP(3);
    const lds_cptr lb = (lds_cptr)lds + lane * 16;
    f32x16 cn;
    { f32x16 s0 = MFMA(lds_b128(lb), qf, (f32x16){}); const float m = xhalf_max(max16(s0));
#pragma unroll
      for (int i = 0; i < 16; ++i) cn[i] = -m; }
    f32x16 o = {};
    f32x16 sA = MFMA(lds_b128(lb), qf, cn), sB = {};
    bf16x8 kn = lds_b128(lb + KVT);
    bf16x8 v0 = lds_b128(lb + 1024), v1 = v0;
    u32x4 p0 = {0u, 0u, 0u, 0u}, p1 = {0u, 0u, 0u, 0u};
    asm volatile("s_nop 15" : A_OPS);
    unsigned b = lds0 + lane * 16; int slot = 0;
#define NCH 8
#pragma unroll 1
    for (int c = 0; c < NCH; ++c) {
        const int sn = slot == 2 ? 0 : slot + 1;
        const unsigned bn = lds0 + lane * 16 + sn * CHB;
        asm volatile(A_TA("%8", 2048, "%8", 6144, "%8", 4096) A_TB("%8", 5120, "%8", 9216, "%8", 7168)
                     A_TA("%8", 8192, "%8", 12288, "%8", 10240) A_TB("%8", 11264, "%8", 15360, "%8", 13312)
                     : A_OPS : "v"(b), "{v[80:95]}"(cn), "{v[96:99]}"(qf) : "memory");
        asm volatile("s_waitcnt vmcnt(0)\n\ts_barrier" ::: "memory");
        if (c + 2 < 8) stage(c + 2, RFL(slot == 0 ? 2 : slot - 1));
        asm volatile(A_TA("%8", 14336, "%8", 18432, "%8", 16384) A_TB("%8", 17408, "%8", 21504, "%8", 19456)
                     A_TA("%8", 20480, "%9", 0, "%8", 22528) A_TB("%8", 23552, "%9", 3072, "%9", 1024)
                     : A_OPS : "v"(b), "v"(bn), "{v[80:95]}"(cn), "{v[96:99]}"(qf) : "memory");
        b = bn; slot = sn;
    }
    asm volatile("s_nop 0\n\t" A_C(119,78,79) "s_waitcnt lgkmcnt(0)\n\ts_nop 1\n\t" A_PV("108:111","116:119") "s_nop 15" : A_OPS :: "memory");
    STAMP(4);
    float mx = fabsf(o[0]);
#pragma unroll
    for (int i = 1; i < 8; ++i) mx = fmaxf(mx, fabsf(o[i]));
    const bool ok = (o[8] < 1.0e18f) && (o[8] > 0.5f) && (mx < 1.0e36f);
    if (__builtin_expect(!__all(ok), 0)) o = attn_exact(kv, qf, lane);
    const float inv = 1.0f / o[8];
    gst16_dev(ws + WS_OF + ((tp * 2 + (w & 1)) * 32 + hg * 4 + ((w >> 1) & 1) * 2 + (w >> 2)) * 1024, lane * 16, pack8(o, 0, inv));
    wait_vm_bar<0>();
    if (threadIdx.x == 0) cnt_add(ws, CNT_O + tp);
}

__global__ __launch_bounds__(512, 4) void k_all(const char* __restrict__ x, const char* __restrict__ wq, const char* __restrict__ wk, const char* __restrict__ wv, const char* __restrict__ wo,
                                                const char* __restrict__ wg, const char* __restrict__ wu, const char* __restrict__ wd, const char* __restrict__ gw,
                                                char* __restrict__ ws, char* __restrict__ out) {
    extern __shared__ __attribute__((aligned(16))) char lds[];
    const int tid = threadIdx.x, lane = tid & 63; const int w = RFL(tid >> 6);
    const int b = blockIdx.x;
    STAMP(0);
    unsigned long long ch = 0;
    if (w == 0) ch = hs_post(ws, lane);
    if (b < 256) {
        attn_role(b, x, wq, wk, wv, ws, lds, w, lane, ch);
    } else {
        const int g = b - 256, xcd = g & 7, i = g >> 3;
        if (i < 28) up_job(4 * (xcd >> 1) + i / 7, 7 * (xcd & 1) + i % 7, x, wg, wu, ws, lds, w, lane, ch);
        else {
            if (w < 4) gate_job(4 * xcd + (i - 28), x, gw, ws, w, lane);
            else if (g == 224 && w == 4) hs_leader(ws, lane);
            if (w == 0) hs_wait(ws, ch, lane);
            wait_vm_bar<0>();
            if (tid == 0) cnt_add(ws, CNT_O + 4 * xcd + (i - 28));
        }
        STAMP(1);
        down_out_job(4 * xcd + (i >> 3), i & 7, i * 8 + w, wd, wo, ws, out, lds, w, lane);
    }
    STAMP(7);
}

extern "C" void kernel_launch(void* const* d_in, const int* in_sizes, int n_in, void* d_out, int out_size, void* d_ws, size_t ws_size, hipStream_t stream) {
    (void)hipFuncSetAttribute((const void*)k_all, hipFuncAttributeMaxDynamicSharedMemorySize, 81920);
    k_all<<<512, 512, 81920, stream>>>((const char*)d_in[0], (const char*)d_in[1], (const char*)d_in[2], (const char*)d_in[3], (const char*)d_in[4],
                                       (const char*)d_in[6], (const char*)d_in[7], (const char*)d_in[8], (const char*)d_in[9], (char*)d_ws, (char*)d_out);
}
```

```cpp
#include <hip/hip_runtime.h>
#include <hip/hip_bf16.h>
#include <stdint.h>

typedef __attribute__((ext_vector_type(8))) short bf16x8;
typedef __attribute__((ext_vector_type(4))) short s16x4;
typedef __attribute__((ext_vector_type(16))) float f32x16;
typedef __attribute__((ext_vector_type(4))) float f32x4;
typedef __attribute__((ext_vector_type(4))) unsigned u32x4;
typedef __attribute__((ext_vector_type(2))) float f32x2;
typedef __attribute__((ext_vector_type(2))) __bf16 bf16x2_t;
typedef __attribute__((address_space(3))) const char* lds_cptr;
typedef __attribute__((address_space(3))) char* lds_ptr;
typedef __attribute__((address_space(1))) unsigned gu32;
typedef __attribute__((address_space(1))) unsigned long long gu64;
typedef short v4i16_t __attribute__((ext_vector_type(4)));
extern "C" __device__ unsigned long long llvm_dispatch_id() __asm("llvm.amdgcn.dispatch.id");
#define DI __device__ __forceinline__
#define MFMA(a, b, c) __builtin_amdgcn_mfma_f32_32x32x16_bf16((a), (b), (c), 0, 0, 0)
#define RLX_AGENT __ATOMIC_RELAXED, __HIP_MEMORY_SCOPE_AGENT
#define RFL(x) __builtin_amdgcn_readfirstlane(x)

#define KVSLEEP 6
#define CNTSLEEP 10
constexpr int HSW = 3;
constexpr int F_ = 1792;
constexpr float QSCALE = 0.25f * 1.4426950408889634f;
constexpr unsigned SPIN_CAP = 1u << 20;

constexpr unsigned WS_CNT = 0;
constexpr unsigned WS_G  = 16384;
constexpr unsigned WS_KV = 32768;
constexpr unsigned WS_REQ = 262144;
constexpr unsigned WS_ECHO = WS_REQ + 65536;
constexpr unsigned WS_OF = 524288;
constexpr unsigned WS_HF = WS_OF + 2097152;
constexpr unsigned WS_DBG = 16u * 1024 * 1024;
constexpr int CNT_H = 0, CNT_O = 16, CNT_KV = 48, CNT_ACK = 49  , GO_KV = 64  ;

#define STAMP(i)

DI unsigned pack_bf16(float lo, float hi) { f32x2 v = {lo, hi}; bf16x2_t r = __builtin_convertvector(v, bf16x2_t); return __builtin_bit_cast(unsigned, r); }
DI void glds(const char* sbase, unsigned voff, unsigned lds_base) {
    unsigned sv;
    asm volatile("s_mov_b32 %0, m0\n\ts_mov_b32 m0, %3\n\ts_nop 0\n\tglobal_load_lds_dwordx4 %1, %2\n\ts_mov_b32 m0, %0"
                 : "=&s"(sv) : "v"(voff), "s"(sbase), "s"(RFL(lds_base)) : "memory");
}
DI void glds_dev(const char* sbase, unsigned voff, unsigned lds_base) {
    unsigned sv;
    asm volatile("s_mov_b32 %0, m0\n\ts_mov_b32 m0, %3\n\ts_nop 0\n\tglobal_load_lds_dwordx4 %1, %2 sc1\n\ts_mov_b32 m0, %0"
                 : "=&s"(sv) : "v"(voff), "s"(sbase), "s"(RFL(lds_base)) : "memory");
}
DI u32x4 gld16_async(const char* sbase, unsigned voff) { u32x4 r; asm volatile("global_load_dwordx4 %0, %1, %2" : "=v"(r) : "v"(voff), "s"(sbase) : "memory"); return r; }
DI unsigned gld4_async(const char* sbase, unsigned voff) { unsigned r; asm volatile("global_load_dword %0, %1, %2" : "=v"(r) : "v"(voff), "s"(sbase) : "memory"); return r; }
DI void gst16_dev(char* sbase, unsigned voff, u32x4 v) { asm volatile("global_store_dwordx4 %0, %1, %2 sc1\n\ts_nop 1" :: "v"(voff), "v"(v), "s"(sbase) : "memory"); }
template <int N> DI void wait_vm_bar() { asm volatile("s_waitcnt vmcnt(%0) lgkmcnt(0)\n\ts_barrier" :: "i"(N) : "memory"); }
DI void lds_bar() { asm volatile("s_waitcnt lgkmcnt(0)\n\ts_barrier" ::: "memory"); }
DI bf16x8 lds_b128(lds_cptr p) { return *(const __attribute__((address_space(3))) bf16x8*)p; }
DI f32x4 lds_f4(lds_cptr p) { return *(const __attribute__((address_space(3))) f32x4*)p; }
DI void lds_st_f4(lds_ptr p, f32x4 v) { *(__attribute__((address_space(3))) f32x4*)p = v; }
DI void lds_st_f(lds_ptr p, float v) { *(__attribute__((address_space(3))) float*)p = v; }
DI float lds_f(lds_cptr p) { return *(const __attribute__((address_space(3))) float*)p; }
DI s16x4 lds_tr(lds_cptr p) { return __builtin_bit_cast(s16x4, __builtin_amdgcn_ds_read_tr16_b64_v4i16((__attribute__((address_space(3))) v4i16_t*)p)); }
DI bf16x8 cat(s16x4 a, s16x4 b) { return __builtin_shufflevector(a, b, 0, 1, 2, 3, 4, 5, 6, 7); }
DI u32x4 pack8(const f32x16& x, int s, float sc) {
    u32x4 p;
    p[0] = pack_bf16(x[8 * s + 0] * sc, x[8 * s + 1] * sc); p[1] = pack_bf16(x[8 * s + 2] * sc, x[8 * s + 3] * sc);
    p[2] = pack_bf16(x[8 * s + 4] * sc, x[8 * s + 5] * sc); p[3] = pack_bf16(x[8 * s + 6] * sc, x[8 * s + 7] * sc);
    return p;
}
DI u32x4 pack44(f32x4 a, f32x4 b, float sc) { u32x4 p; p[0] = pack_bf16(a[0] * sc, a[1] * sc); p[1] = pack_bf16(a[2] * sc, a[3] * sc); p[2] = pack_bf16(b[0] * sc, b[1] * sc); p[3] = pack_bf16(b[2] * sc, b[3] * sc); return p; }

DI gu32* cnt_ptr(char* ws, int idx) { return (gu32*)(ws + WS_CNT + idx * 128); }
DI void cnt_add(char* ws, int idx) { __hip_atomic_fetch_add(cnt_ptr(ws, idx), 1u, RLX_AGENT); }
DI unsigned long long hs_post(char* ws, int lane) {
    const unsigned long long ch = __builtin_amdgcn_s_memrealtime() * 2 + 1;
    if (lane == 0) __hip_atomic_store((gu64*)(ws + WS_REQ + blockIdx.x * 128), ch, RLX_AGENT);
    return ch;
}
DI void hs_wait(char* ws, unsigned long long ch, int lane) {
    for (unsigned s = 0; s < SPIN_CAP; ++s) {
        const unsigned long long e = __hip_atomic_load((gu64*)(ws + WS_ECHO + blockIdx.x * 128), RLX_AGENT);
        if ((unsigned)RFL((unsigned)e) == (unsigned)ch && (unsigned)RFL((unsigned)(e >> 32)) == (unsigned)(ch >> 32)) break;
        __builtin_amdgcn_s_sleep(4);
    }
    if (lane == 0) cnt_add(ws, CNT_ACK + (blockIdx.x >> 6));
}
template <int SLP> DI void cnt_wait(char* ws, int idx, unsigned need) {
    for (unsigned s = 0; s < SPIN_CAP; ++s) {
        if ((unsigned)RFL(__hip_atomic_load(cnt_ptr(ws, idx), RLX_AGENT)) >= need) break;
        __builtin_amdgcn_s_sleep(SLP);
    }
}
typedef __attribute__((address_space(3))) unsigned lu32;
constexpr int LCNT = 81904;
#define RLX_WG __ATOMIC_RELAXED, __HIP_MEMORY_SCOPE_WORKGROUP
DI void lcnt_wait(char* lds, int idx, unsigned need) {
    lu32* c = (lu32*)((lds_ptr)lds + LCNT) + idx;
    for (unsigned s = 0; s < SPIN_CAP * 16; ++s) { if ((unsigned)RFL(__hip_atomic_load(c, RLX_WG)) >= need) break; __builtin_amdgcn_s_sleep(1); }
    asm volatile("" ::: "memory");
}
DI void lcnt_arrive(char* lds, int idx, int lane) {
    asm volatile("s_waitcnt lgkmcnt(0)" ::: "memory");
    if (lane == 0) __hip_atomic_fetch_add((lu32*)((lds_ptr)lds + LCNT) + idx, 1u, RLX_WG);
}
DI void hs_leader(char* ws, char* lds, int j, int lane) {
    if (j == 0) {
        __hip_atomic_store(cnt_ptr(ws, lane), 0u, RLX_AGENT); __hip_atomic_store(cnt_ptr(ws, 64 + lane), 0u, RLX_AGENT);
        asm volatile("s_waitcnt vmcnt(0)" ::: "memory");
        lcnt_arrive(lds, 3, lane);
    } else lcnt_wait(lds, 3, 1u);
    unsigned long long p0 = 0ull, p1 = 0ull;
    const unsigned long long t0 = __builtin_amdgcn_s_memrealtime() * 2;
    const unsigned o = (j * 128 + lane) * 128;
    for (unsigned s = 0; s < SPIN_CAP; ++s) {
        const unsigned long long r0 = __hip_atomic_load((gu64*)(ws + WS_REQ + o), RLX_AGENT), r1 = __hip_atomic_load((gu64*)(ws + WS_REQ + o + 8192), RLX_AGENT);
        if (r0 != p0) __hip_atomic_store((gu64*)(ws + WS_ECHO + o), r0, RLX_AGENT);
        if (r1 != p1) __hip_atomic_store((gu64*)(ws + WS_ECHO + o + 8192), r1, RLX_AGENT);
        p0 = r0; p1 = r1;
        const bool recent = (r0 + 1000ull - t0) < 4000ull && (r1 + 1000ull - t0) < 4000ull;
        if (__all(recent) || s > 16) {
            const unsigned a = __hip_atomic_load(cnt_ptr(ws, CNT_ACK + 2 * j + (lane & 1)), RLX_AGENT);
            if (__all(a == 64u)) break;
            __builtin_amdgcn_s_sleep(64);
        }
    }
    __builtin_amdgcn_s_waitcnt(0x0F70);
}

DI void kv_tile(int tile, const char* x, const char* wk, const char* wv, char* ws, char* lds, int w, int lane, unsigned long long ch) {
    const int h = lane >> 5, r32 = lane & 31;
    const unsigned sb = (unsigned)(uintptr_t)lds + w * 16384;
    { const int row = lane >> 3; const unsigned xo = (tile * 32 + row) * 1024 + w * 256;
      const unsigned wo_ = w * 4096 + lane * 16;
#pragma unroll
      for (int c = 0; c < 2; ++c) {
#pragma unroll
          for (int i = 0; i < 4; ++i) glds(x + i * 8192 + c * 128, xo + (((lane & 7) ^ ((i * 4 + (row >> 1)) & 7)) * 16), sb + c * 8192 + i * 1024);
          glds(wv + c * 2048, wo_, sb + c * 8192 + 4096); glds(wv + c * 2048 + 1024, wo_, sb + c * 8192 + 5120);
          glds(wk + c * 2048, wo_, sb + c * 8192 + 6144); glds(wk + c * 2048 + 1024, wo_, sb + c * 8192 + 7168); } }
    STAMP(11);
    f32x16 a = {};
    const lds_cptr xb = (lds_cptr)lds + w * 16384 + r32 * 128;
    const lds_cptr wb = (lds_cptr)lds + w * 16384 + 4096 + ((lane >> 4) & 1) * 2048 + (8 * h + ((lane & 15) >> 2)) * 32 + (lane & 3) * 8;
#pragma unroll
    for (int c = 0; c < 2; ++c) {
        if (c == 0) asm volatile("s_waitcnt vmcnt(8)" ::: "memory"); else asm volatile("s_waitcnt vmcnt(0)" ::: "memory");
#pragma unroll
        for (int ks = 0; ks < 4; ++ks) a = MFMA(cat(lds_tr(wb + c * 8192 + ks * 512), lds_tr(wb + c * 8192 + ks * 512 + 128)), lds_b128(xb + c * 8192 + (((2 * ks + h) ^ ((r32 >> 1) & 7)) * 16)), a);
    }
    STAMP(12);
    { lds_ptr p = (lds_ptr)lds + w * 16384;
      lds_st_f4(p + lane * 16, (f32x4){a[8], a[9], a[10], a[11]}); lds_st_f4(p + 1024 + lane * 16, (f32x4){a[12], a[13], a[14], a[15]});
#pragma unroll
      for (int r = 0; r < 8; ++r) lds_st_f(p + 2048 + ((r & 3) + 8 * (r >> 2) + 4 * h) * 144 + r32 * 4, a[r]); }
    lcnt_arrive(lds, 0, lane);
    if (w < 3) {
        lcnt_wait(lds, 0, 4u);
        f32x4 s0 = {}, s1 = {};
        const lds_cptr p = (lds_cptr)lds + (w == 0 ? lane * 16 : 2048 + (r32 & 15) * 144 + h * 16 + (w - 1) * 64);
        const int d1 = w == 0 ? 1024 : 32;
#pragma unroll
        for (int q = 0; q < 4; ++q) { s0 += lds_f4(p + q * 16384); s1 += lds_f4(p + q * 16384 + d1); }
        u32x4 v = pack44(s0, s1, 1.0f);
        if (w > 0 && r32 >= 16) { unsigned c = (r32 == 16 || r32 == 20) ? 0x3F803F80u : 0u; v = (u32x4){c, c, c, c}; }
        gst16_dev(ws + WS_KV + tile * 3072 + w * 1024, lane * 16, v);
        STAMP(13);
        asm volatile("s_waitcnt vmcnt(0)" ::: "memory");
        lcnt_arrive(lds, 1, lane);
    } else {
        hs_wait(ws, ch, lane);
        STAMP(13);
        lcnt_wait(lds, 1, 3u);
        STAMP(14);
        unsigned kvn = 0;
        if (lane == 0) kvn = __hip_atomic_fetch_add(cnt_ptr(ws, CNT_KV), 1u, RLX_AGENT);
        if ((unsigned)RFL(kvn) == 63u && lane < 32) __hip_atomic_store(cnt_ptr(ws, GO_KV + lane), 1u, RLX_AGENT);
        STAMP(15);
    }
}

struct T64 { unsigned xs, ws; int xoff[2], woff[2][2]; };
template <int KTOT, bool XFRAG, bool PERMN>
DI T64 t64_setup(int tok0, int n0, int w, int lane) {
    T64 t;
    if (XFRAG) t.xs = (((tok0 >> 5) + (w >> 2)) * (KTOT / 16) + (w & 3)) * 1024 + lane * 16;
    else { int row = 8 * w + (lane >> 3); int ch = (lane & 7) ^ ((row >> 1) & 7); t.xs = (tok0 + row) * 1024 + ch * 16; }
    { int row = 8 * w + (lane >> 3); int ch = (lane & 7) ^ (((row >> 1) & 1) << 2); t.ws = row * 1024 + n0 * 2 + ch * 16; }
    const int ti = w & 1, ni = (w >> 1) & 1, kh = w >> 2, h = lane >> 5, r32 = lane & 31;
#pragma unroll
    for (int i = 0; i < 2; ++i) {
        const int ks = 2 * kh + i;
        if (XFRAG) t.xoff[i] = (ti * 4 + ks) * 1024 + lane * 16;
        else { int tok = ti * 32 + r32; t.xoff[i] = tok * 128 + (((2 * ks + h) ^ ((tok >> 1) & 7)) * 16); }
        const int g = (lane >> 4) & 1, q = (lane & 15) >> 2, p = lane & 3;
        const int pp = PERMN ? (((p & 1) << 1) | (p >> 1)) : p;
        const int col = ni * 32 + g * 16 + 4 * pp;
#pragma unroll
        for (int tt = 0; tt < 2; ++tt) {
            const int row = XFRAG ? (16 * ks + 8 * tt + 4 * h + q) : (16 * ks + 8 * h + 4 * tt + q);
            t.woff[i][tt] = row * 128 + (((col >> 3) ^ (((row >> 1) & 1) << 2)) * 16) + (col & 7) * 2;
        }
    }
    return t;
}
DI void t64_compute(const T64& t, lds_cptr b, f32x16& acc) {
#pragma unroll
    for (int i = 0; i < 2; ++i) {
        bf16x8 xf = lds_b128(b + t.xoff[i]);
        bf16x8 wf = cat(lds_tr(b + 8192 + t.woff[i][0]), lds_tr(b + 8192 + t.woff[i][1]));
        acc = MFMA(wf, xf, acc);
    }
}
DI void t64_xchg_write(const f32x16& acc, lds_ptr scr, int w, int lane) {
    const int kh = w >> 2;
    f32x4 a = kh ? (f32x4){acc[0], acc[1], acc[2], acc[3]} : (f32x4){acc[8], acc[9], acc[10], acc[11]};
    f32x4 b = kh ? (f32x4){acc[4], acc[5], acc[6], acc[7]} : (f32x4){acc[12], acc[13], acc[14], acc[15]};
    lds_st_f4(scr + w * 2048 + lane * 16, a); lds_st_f4(scr + w * 2048 + 1024 + lane * 16, b);
}
DI void t64_xchg_read(const f32x16& acc, lds_cptr scr, int w, int lane, f32x4& r0, f32x4& r1) {
    const int kh = w >> 2;
    r0 = kh ? (f32x4){acc[8], acc[9], acc[10], acc[11]} : (f32x4){acc[0], acc[1], acc[2], acc[3]};
    r1 = kh ? (f32x4){acc[12], acc[13], acc[14], acc[15]} : (f32x4){acc[4], acc[5], acc[6], acc[7]};
    r0 += lds_f4(scr + (w ^ 4) * 2048 + lane * 16); r1 += lds_f4(scr + (w ^ 4) * 2048 + 1024 + lane * 16);
}

DI float bf_lo(unsigned u) { return __uint_as_float(u << 16); }
DI float bf_hi(unsigned u) { return __uint_as_float(u & 0xffff0000u); }
DI void gate_job(int tp, const char* x, const char* gw, char* ws, int w, int lane) {
    const u32x4 gv = *(const u32x4*)(gw + lane * 16);
    float res = 0.f;
#pragma unroll 4
    for (int t = 0; t < 16; ++t) {
        const u32x4 xv = *(const u32x4*)(x + (size_t)(tp * 64 + w * 16 + t) * 1024 + lane * 16);
        float s = 0.f;
#pragma unroll
        for (int i = 0; i < 4; ++i) s += bf_lo(xv[i]) * bf_lo(gv[i]) + bf_hi(xv[i]) * bf_hi(gv[i]);
#pragma unroll
        for (int o = 32; o >= 1; o >>= 1) s += __shfl_xor(s, o);
        if (lane == t) res = s;
    }
    if (lane < 16) __hip_atomic_store((gu32*)(ws + WS_G) + tp * 64 + w * 16 + lane, __float_as_uint(1.0f / (1.0f + __expf(-res))), RLX_AGENT);
    __builtin_amdgcn_s_waitcnt(0x0F70);
}

DI void up_job(int rp, int cp, const char* x, const char* wg, const char* wu, char* ws, char* lds, int w, int lane, unsigned long long ch) {
    constexpr int STB = 24576, NST = 16;
    const unsigned lds0 = (unsigned)(uintptr_t)lds + w * 1024;
    unsigned xs, gs;
    { int row = 16 * w + (lane >> 2); int c = (lane & 3) ^ ((row >> 2) & 3); xs = (rp * 128 + row) * 1024 + c * 16; }
    { int row = 4 * w + (lane >> 4); int c = (lane & 15) ^ ((row & 3) << 2); gs = row * (F_ * 2) + cp * 256 + c * 16; }
#define UP_STAGE(s) do { const unsigned b_ = lds0 + ((s) % 3) * STB; glds(x + (s) * 64, xs, b_); glds(wg + (s) * 32 * F_ * 2, gs, b_ + 8192); glds(wu + (s) * 32 * F_ * 2, gs, b_ + 16384); } while (0)
    UP_STAGE(0); UP_STAGE(1);
    const int th = w & 1, fg = w >> 1, h = lane >> 5, r32 = lane & 31;
    lds_cptr xb[2], wb;
#pragma unroll
    for (int ks = 0; ks < 2; ++ks) { int tok = th * 64 + r32; xb[ks] = (lds_cptr)lds + tok * 64 + (((2 * ks + h) ^ ((tok >> 2) & 3)) * 16); }
    { const int g = (lane >> 4) & 1, q = (lane & 15) >> 2, p = lane & 3; const int col = fg * 32 + g * 16 + 4 * p;
      wb = (lds_cptr)lds + 8192 + (8 * h + q) * 256 + (((col >> 3) ^ (q << 2)) * 16) + (col & 7) * 2; }
    f32x16 ag[2] = {}, au[2] = {};
#pragma unroll
    for (int s = 0; s < NST; ++s) {
        if (s + 1 < NST) wait_vm_bar<3>(); else wait_vm_bar<0>();
        if ((s & 3) == 0) STAMP(8 + (s >> 2));
        if (s + 2 < NST) UP_STAGE(s + 2);
        const int so = (s % 3) * STB;
#pragma unroll
        for (int ks = 0; ks < 2; ++ks) {
            bf16x8 x0 = lds_b128(xb[ks] + so), x1 = lds_b128(xb[ks] + so + 2048);
            bf16x8 gf = cat(lds_tr(wb + so + ks * 4096), lds_tr(wb + so + ks * 4096 + 1024));
            bf16x8 uf = cat(lds_tr(wb + so + 8192 + ks * 4096), lds_tr(wb + so + 8192 + ks * 4096 + 1024));
            ag[0] = MFMA(gf, x0, ag[0]); ag[1] = MFMA(gf, x1, ag[1]);
            au[0] = MFMA(uf, x0, au[0]); au[1] = MFMA(uf, x1, au[1]);
        }
    }
#undef UP_STAGE
    STAMP(2);
#pragma unroll
    for (int i = 0; i < 2; ++i) {
        f32x16 hh = ag[i] * au[i];
        char* dst = ws + WS_HF + ((rp * 4 + th * 2 + i) * 112 + cp * 8 + fg * 2) * 1024;
        gst16_dev(dst, lane * 16, pack8(hh, 0, 1.0f));
        gst16_dev(dst, 1024 + lane * 16, pack8(hh, 1, 1.0f));
    }
    if (w == HSW) hs_wait(ws, ch, lane);
    wait_vm_bar<0>();
    if (threadIdx.x == HSW * 64) cnt_add(ws, CNT_H + rp);
}

DI void down_out_job(int tp, int cg, int jw, const char* wd, const char* wo, char* ws, char* out, char* lds, int w, int lane) {
    constexpr int NS = 5, NST = 28;
    const unsigned lds0 = (unsigned)(uintptr_t)lds + w * 1024;
    const T64 t = t64_setup<1792, true, true>(tp * 64, cg * 64, w, lane);
    const char* hf = ws + WS_HF;
    const unsigned tch = gld4_async(wd, (jw * 56 + (lane < 56 ? lane : 0)) * 128);
#pragma unroll
    for (int s = 0; s < 5; ++s) glds(wd + s * 65536, t.ws, lds0 + s * 16384 + 8192);
    if (w == HSW) cnt_wait<CNTSLEEP>(ws, CNT_H + (tp >> 1), 14u);
    wait_vm_bar<0>();
    asm volatile("" :: "v"(tch));
    STAMP(3);
#pragma unroll
    for (int s = 0; s < 5; ++s) glds_dev(hf + s * 4096, t.xs, lds0 + s * 16384);
    unsigned tch2; asm volatile("global_load_dword %0, %1, %2 sc1" : "=v"(tch2) : "v"(tp * 229376 + ((cg * 8 + w) * 28 + (lane < 28 ? lane : 0)) * 128), "s"(hf) : "memory");
    f32x16 acc = {};
#define D_STAGE(s) do { const unsigned b_ = lds0 + ((s) % NS) * 16384; glds_dev(hf + (s) * 4096, t.xs, b_); glds(wd + (s) * 65536, t.ws, b_ + 8192); } while (0)
#pragma unroll
    for (int k = 0; k < NST / 2; ++k) {
        if (k == 0) wait_vm_bar<4>(); else if (k == 1) wait_vm_bar<2>(); else if (k < NST / 2 - 1) wait_vm_bar<2>(); else wait_vm_bar<0>();
        if (k == 2) asm volatile("" :: "v"(tch2));
        if (k >= 1) { if (2 * k + 3 < NST) D_STAGE(2 * k + 3); if (2 * k + 4 < NST) D_STAGE(2 * k + 4); }
        t64_compute(t, (lds_cptr)lds + ((2 * k) % NS) * 16384, acc);
        t64_compute(t, (lds_cptr)lds + ((2 * k + 1) % NS) * 16384, acc);
    }
#undef D_STAGE
    STAMP(4);
    lds_bar();
#pragma unroll
    for (int s = 0; s < 8; ++s) glds(wo + s * 65536, t.ws, lds0 + s * 8192);
    wait_vm_bar<0>();
    bf16x8 wf[16];
#pragma unroll
    for (int s = 0; s < 8; ++s)
#pragma unroll
        for (int i = 0; i < 2; ++i) wf[2 * s + i] = cat(lds_tr((lds_cptr)lds + s * 8192 + t.woff[i][0]), lds_tr((lds_cptr)lds + s * 8192 + t.woff[i][1]));
    if (w == HSW) cnt_wait<CNTSLEEP>(ws, CNT_O + tp, 9u);
    lds_bar();
    STAMP(5);
    const unsigned os = ((tp * 2 + (w >> 2)) * 32 + (w & 3)) * 1024 + lane * 16;
#pragma unroll
    for (int s = 0; s < 8; ++s) glds_dev(ws + WS_OF + s * 4096, os, lds0 + s * 8192);
    const int tok = tp * 64 + (w & 1) * 32 + (lane & 31);
    unsigned gu; asm volatile("global_load_dword %0, %1, %2 sc1" : "=v"(gu) : "v"(tok * 4), "s"(ws + WS_G) : "memory");
    f32x16 acca = {};
#pragma unroll
    for (int s = 0; s < 8; ++s) {
        if (s == 0) wait_vm_bar<5>(); else if (s == 4) wait_vm_bar<1>();
#pragma unroll
        for (int i = 0; i < 2; ++i) acca = MFMA(wf[2 * s + i], lds_b128((lds_cptr)lds + s * 8192 + t.xoff[i]), acca);
    }
    asm volatile("s_waitcnt vmcnt(0)" : "+v"(gu) :: "memory");
    const float g = __uint_as_float(gu), g1 = 1.0f - g;
#pragma unroll
    for (int i = 0; i < 16; ++i) acc[i] = g * acca[i] + g1 * acc[i];
    t64_xchg_write(acc, (lds_ptr)lds + 65536, w, lane);
    lds_bar();
    f32x4 r0, r1; t64_xchg_read(acc, (lds_cptr)lds + 65536, w, lane, r0, r1);
    *(u32x4*)(out + (size_t)tok * 1024 + (cg * 64 + ((w >> 1) & 1) * 32 + (w >> 2) * 16 + 8 * (lane >> 5)) * 2) = pack44(r0, r1, 1.0f);
}

constexpr int KVT = 3072, CHT = 8, CHB = CHT * KVT;
DI float xhalf_max(float m) { auto rr = __builtin_amdgcn_permlane32_swap(__float_as_uint(m), __float_as_uint(m), false, false); return fmaxf(__uint_as_float(rr[0]), __uint_as_float(rr[1])); }
DI float max16(const f32x16& s) {
    float a = fmaxf(fmaxf(s[0], s[1]), s[2]), b = fmaxf(fmaxf(s[3], s[4]), s[5]);
    a = fmaxf(fmaxf(a, s[6]), s[7]); b = fmaxf(fmaxf(b, s[8]), s[9]); a = fmaxf(fmaxf(a, s[10]), s[11]); b = fmaxf(fmaxf(b, s[12]), s[13]);
    a = fmaxf(fmaxf(a, s[14]), s[15]); return fmaxf(a, b);
}
DI u32x4 exp_pack(const f32x16& s, int o) {
    u32x4 p;
#pragma unroll
    for (int i = 0; i < 4; ++i) p[i] = pack_bf16(__builtin_amdgcn_exp2f(s[o + 2 * i]), __builtin_amdgcn_exp2f(s[o + 2 * i + 1]));
    return p;
}
DI f32x16 attn_exact(const char* kv, bf16x8 qf, int lane) {
    f32x16 o = {}; float m = -INFINITY;
#pragma unroll 1
    for (int t = 0; t < 64; ++t) {
        u32x4 kf, v0, v1;
        asm volatile("global_load_dwordx4 %0, %3, %4 sc1\n\tglobal_load_dwordx4 %1, %3, %4 offset:1024 sc1\n\tglobal_load_dwordx4 %2, %3, %4 offset:2048 sc1\n\ts_waitcnt vmcnt(0)"
                     : "=&v"(kf), "=&v"(v0), "=&v"(v1) : "v"(t * KVT + lane * 16), "s"(kv) : "memory");
        f32x16 s = MFMA(__builtin_bit_cast(bf16x8, kf), qf, (f32x16){});
        const float mn = fmaxf(m, xhalf_max(max16(s)));
        const float al = __builtin_amdgcn_exp2f(m - mn);
        m = mn;
#pragma unroll
        for (int i = 0; i < 16; ++i) { o[i] *= al; s[i] -= mn; }
        o = MFMA(__builtin_bit_cast(bf16x8, v0), __builtin_bit_cast(bf16x8, exp_pack(s, 0)), o);
        o = MFMA(__builtin_bit_cast(bf16x8, v1), __builtin_bit_cast(bf16x8, exp_pack(s, 8)), o);
    }
    return o;
}
#define A_E(r) "v_exp_f32 v" #r ", v" #r "\n\t"
#define A_C(d, a, b) "v_cvt_pk_bf16_f32 v" #d ", v" #a ", v" #b "\n\t"
#define A_QK(d) "v_mfma_f32_32x32x16_bf16 v[" d "], v[100:103], v[96:99], v[80:95]\n\t"
#define A_PV(a, p) "v_mfma_f32_32x32x16_bf16 v[32:47], v[" a "], v[" p "], v[32:47]\n\t"
#define A_W "s_waitcnt lgkmcnt(2)\n\t"
#define A_RD(d, base, off) "ds_read_b128 v[" d "], " base " offset:" #off "\n\t"
#define A_T(S0,S1,S2,S3,S4,S5,S6,S7,S8,S9,S10,S11,S12,S13,S14,S15,P14,P15,SN, BV1,OV1, BK,OK, BV0,OV0) \
    A_E(S0) A_C(119,P14,P15) A_E(S1) A_E(S2) A_W A_PV("108:111","116:119") A_RD("108:111", BV1, OV1) A_E(S3) A_C(112,S0,S1) A_E(S4) A_E(S5) A_C(113,S2,S3) \
    A_W A_QK(SN) A_RD("100:103", BK, OK) A_E(S6) A_E(S7) A_C(114,S4,S5) A_E(S8) A_E(S9) A_C(115,S6,S7) A_E(S10) A_E(S11) \
    A_W A_PV("104:107","112:115") A_RD("104:107", BV0, OV0) A_C(116,S8,S9) A_E(S12) A_E(S13) A_C(117,S10,S11) A_E(S14) A_E(S15) A_C(118,S12,S13)
#define A_TA(BV1,OV1,BK,OK,BV0,OV0) A_T(48,49,50,51,52,53,54,55,56,57,58,59,60,61,62,63,78,79,"64:79",BV1,OV1,BK,OK,BV0,OV0)
#define A_TB(BV1,OV1,BK,OK,BV0,OV0) A_T(64,65,66,67,68,69,70,71,72,73,74,75,76,77,78,79,62,63,"48:63",BV1,OV1,BK,OK,BV0,OV0)
#define A_OPS "+{v[32:47]}"(o), "+{v[48:63]}"(sA), "+{v[64:79]}"(sB), "+{v[100:103]}"(kn), "+{v[104:107]}"(v0), "+{v[108:111]}"(v1), "+{v[112:115]}"(p0), "+{v[116:119]}"(p1)
DI void attn_role(int blk, const char* x, const char* wq, const char* wk, const char* wv, char* ws, char* lds, int w, int lane, unsigned long long ch) {
    const int xcd = blk & 7, tp = xcd * 4 + ((blk >> 3) & 3), hg = blk >> 5;
    const unsigned lds0 = (unsigned)(uintptr_t)lds;
    const T64 t = t64_setup<512, false, false>(tp * 64, hg * 64, w, lane);
#define Q_STAGE(s) do { const unsigned b_ = lds0 + w * 1024 + (((s) + 4) % 5) * 16384; glds(x + (s) * 128, t.xs, b_); glds(wq + (s) * 65536, t.ws, b_ + 8192); } while (0)
#define Q_COMP(s) t64_compute(t, (lds_cptr)lds + (((s) + 4) % 5) * 16384, acc)
    if (hg == 7) {
        if (threadIdx.x < 4) *((lu32*)((lds_ptr)lds + LCNT) + threadIdx.x) = 0u;
        lds_bar();
        if (w < 4) kv_tile(tp * 2, x, wk, wv, ws, lds, w, lane, ch);
        lds_bar();
    }
    Q_STAGE(0); Q_STAGE(1); Q_STAGE(2); Q_STAGE(3); Q_STAGE(4);
    f32x16 acc = {};
    STAMP(9);
    wait_vm_bar<8>(); STAMP(10);
    Q_COMP(0);
    wait_vm_bar<6>(); Q_STAGE(5); Q_COMP(1);
    wait_vm_bar<6>(); Q_STAGE(6); Q_COMP(2);
    wait_vm_bar<6>(); Q_STAGE(7); Q_COMP(3);
    wait_vm_bar<6>(); Q_COMP(4);
    wait_vm_bar<4>(); Q_COMP(5);
    wait_vm_bar<2>(); Q_COMP(6);
    wait_vm_bar<0>(); Q_COMP(7);
#undef Q_STAGE
#undef Q_COMP
    t64_xchg_write(acc, (lds_ptr)lds + 49152, w, lane);
    lds_bar();
    f32x4 r0, r1; t64_xchg_read(acc, (lds_cptr)lds + 49152, w, lane, r0, r1);
    const bf16x8 qf = __builtin_bit_cast(bf16x8, pack44(r0, r1, QSCALE));
    STAMP(1);
    if (w == HSW) {
        if (hg != 7) hs_wait(ws, ch, lane);
        STAMP(8);
        cnt_wait<KVSLEEP>(ws, GO_KV + (blk & 31), 1u);
    }
    lds_bar();
    STAMP(2);
    const char* kv = ws + WS_KV;
    const unsigned kvo = w * KVT + lane * 16;
    auto stage = [&](int c, int slot) {
        const unsigned b = lds0 + slot * CHB + w * KVT; const char* s = kv + c * CHB;
        glds_dev(s, kvo, b); glds_dev(s + 1024, kvo, b + 1024); glds_dev(s + 2048, kvo, b + 2048);
    };
    stage(0, 0); stage(1, 1);
    asm volatile("s_waitcnt vmcnt(3)\n\ts_barrier" ::: "memory");
    STAMP(3);
    const lds_cptr lb = (lds_cptr)lds + lane * 16;
    f32x16 cn;
    { f32x16 s0 = MFMA(lds_b128(lb), qf, (f32x16){}); const float m = xhalf_max(max16(s0));
#pragma unroll
      for (int i = 0; i < 16; ++i) cn[i] = -m; }
    f32x16 o = {};
    f32x16 sA = MFMA(lds_b128(lb), qf, cn), sB = {};
    bf16x8 kn = lds_b128(lb + KVT);
    bf16x8 v0 = lds_b128(lb + 1024), v1 = v0;
    u32x4 p0 = {0u, 0u, 0u, 0u}, p1 = {0u, 0u, 0u, 0u};
    asm volatile("s_nop 15" : A_OPS);
    unsigned b = lds0 + lane * 16; int slot = 0;
#define NCH 8
#pragma unroll 1
    for (int c = 0; c < NCH; ++c) {
        const int sn = slot == 2 ? 0 : slot + 1;
        const unsigned bn = lds0 + lane * 16 + sn * CHB;
        asm volatile(A_TA("%8", 2048, "%8", 6144, "%8", 4096) A_TB("%8", 5120, "%8", 9216, "%8", 7168)
                     A_TA("%8", 8192, "%8", 12288, "%8", 10240) A_TB("%8", 11264, "%8", 15360, "%8", 13312)
                     : A_OPS : "v"(b), "{v[80:95]}"(cn), "{v[96:99]}"(qf) : "memory");
        asm volatile("s_waitcnt vmcnt(0)\n\ts_barrier" ::: "memory");
        if (c + 2 < 8) stage(c + 2, RFL(slot == 0 ? 2 : slot - 1));
        asm volatile(A_TA("%8", 14336, "%8", 18432, "%8", 16384) A_TB("%8", 17408, "%8", 21504, "%8", 19456)
                     A_TA("%8", 20480, "%9", 0, "%8", 22528) A_TB("%8", 23552, "%9", 3072, "%9", 1024)
                     : A_OPS : "v"(b), "v"(bn), "{v[80:95]}"(cn), "{v[96:99]}"(qf) : "memory");
        b = bn; slot = sn;
    }
    asm volatile("s_nop 0\n\t" A_C(119,78,79) "s_waitcnt lgkmcnt(0)\n\ts_nop 1\n\t" A_PV("108:111","116:119") "s_nop 15" : A_OPS :: "memory");
    STAMP(4);
    float mx = fabsf(o[0]);
#pragma unroll
    for (int i = 1; i < 8; ++i) mx = fmaxf(mx, fabsf(o[i]));
    const bool ok = (o[8] < 1.0e18f) && (o[8] > 0.5f) && (mx < 1.0e36f);
    if (__builtin_expect(!__all(ok), 0)) o = attn_exact(kv, qf, lane);
    const float inv = 1.0f / o[8];
    gst16_dev(ws + WS_OF + ((tp * 2 + (w & 1)) * 32 + hg * 4 + ((w >> 1) & 1) * 2 + (w >> 2)) * 1024, lane * 16, pack8(o, 0, inv));
    wait_vm_bar<0>();
    if (threadIdx.x == HSW * 64) cnt_add(ws, CNT_O + tp);
}

__global__ __launch_bounds__(512, 4) void k_all(const char* __restrict__ x, const char* __restrict__ wq, const char* __restrict__ wk, const char* __restrict__ wv, const char* __restrict__ wo,
                                                const char* __restrict__ wg, const char* __restrict__ wu, const char* __restrict__ wd, const char* __restrict__ gw,
                                                char* __restrict__ ws, char* __restrict__ out) {
    extern __shared__ __attribute__((aligned(16))) char lds[];
    const int tid = threadIdx.x, lane = tid & 63; const int w = RFL(tid >> 6);
    const int b = blockIdx.x;
    STAMP(0);
    unsigned long long ch = 0;
    if (w == HSW) ch = hs_post(ws, lane);
    if (b < 256) {
        attn_role(b, x, wq, wk, wv, ws, lds, w, lane, ch);
    } else {
        const int g = b - 256, xcd = g & 7, i = g >> 3;
        if (i < 28) up_job(4 * (xcd >> 1) + i / 7, 7 * (xcd & 1) + i % 7, x, wg, wu, ws, lds, w, lane, ch);
        else {
            if (tid < 4) *((lu32*)((lds_ptr)lds + LCNT) + tid) = 0u;
            lds_bar();
            if (w < 4) { kv_tile((4 * xcd + (i - 28)) * 2 + 1, x, wk, wv, ws, lds, w, lane, ch); gate_job(4 * xcd + (i - 28), x, gw, ws, w, lane); }
            else if (g == 224) hs_leader(ws, lds, w - 4, lane);
            wait_vm_bar<0>();
            if (tid == HSW * 64) cnt_add(ws, CNT_O + 4 * xcd + (i - 28));
        }
        STAMP(1);
        down_out_job(4 * xcd + (i >> 3), i & 7, i * 8 + w, wd, wo, ws, out, lds, w, lane);
        if (g == 0) {
            __syncthreads();
            if (tid == 0) {
                const unsigned long long id = llvm_dispatch_id() + 0x1234567800000000ull;
                gu64* p = (gu64*)(ws + WS_DBG);
                const unsigned long long old = __hip_atomic_load(p, RLX_AGENT);
                __hip_atomic_store(p, id, RLX_AGENT);
                if (old == id) *(volatile unsigned*)out = 0x7fc07fc0u;
            }
        }
    }
    STAMP(7);
}

extern "C" void kernel_launch(void* const* d_in, const int* in_sizes, int n_in, void* d_out, int out_size, void* d_ws, size_t ws_size, hipStream_t stream) {
    (void)hipFuncSetAttribute((const void*)k_all, hipFuncAttributeMaxDynamicSharedMemorySize, 81920);
    k_all<<<512, 512, 81920, stream>>>((const char*)d_in[0], (const char*)d_in[1], (const char*)d_in[2], (const char*)d_in[3], (const char*)d_in[4],
                                       (const char*)d_in[6], (const char*)d_in[7], (const char*)d_in[8], (const char*)d_in[9], (char*)d_ws, (char*)d_out);
}
```

```cpp
#include <hip/hip_runtime.h>
#include <hip/hip_bf16.h>
#include <stdint.h>

typedef __attribute__((ext_vector_type(8))) short bf16x8;
typedef __attribute__((ext_vector_type(4))) short s16x4;
typedef __attribute__((ext_vector_type(16))) float f32x16;
typedef __attribute__((ext_vector_type(4))) float f32x4;
typedef __attribute__((ext_vector_type(4))) unsigned u32x4;
typedef __attribute__((ext_vector_type(2))) float f32x2;
typedef __attribute__((ext_vector_type(2))) __bf16 bf16x2_t;
typedef __attribute__((address_space(3))) const char* lds_cptr;
typedef __attribute__((address_space(3))) char* lds_ptr;
typedef __attribute__((address_space(1))) unsigned gu32;
typedef __attribute__((address_space(1))) unsigned long long gu64;
typedef short v4i16_t __attribute__((ext_vector_type(4)));
extern "C" __device__ unsigned long long llvm_dispatch_id() __asm("llvm.amdgcn.dispatch.id");
#define DI __device__ __forceinline__
#define MFMA(a, b, c) __builtin_amdgcn_mfma_f32_32x32x16_bf16((a), (b), (c), 0, 0, 0)
#define RLX_AGENT __ATOMIC_RELAXED, __HIP_MEMORY_SCOPE_AGENT
#define RFL(x) __builtin_amdgcn_readfirstlane(x)

#define KVSLEEP 6
#define CNTSLEEP 10
constexpr int HSW = 0;
#define NREP 8
#define OSLEEP 4
#define DPRIO 0
#define KVHG 6
#define UPROT 4
#define UPDEEP 0
#define QROT 1
#define SPLITBAR 0
#define GX 0
#define TCH2 1
#define DROT 1
constexpr int F_ = 1792;
constexpr float QSCALE = 0.25f * 1.4426950408889634f;
constexpr unsigned SPIN_CAP = 1u << 20;

constexpr unsigned WS_KVF = 0;
constexpr unsigned WS_G  = 4096;
constexpr unsigned WS_HL = 16384;
constexpr unsigned WS_OL = WS_HL + 32768;
constexpr unsigned WS_KV = 131072;
constexpr unsigned WS_OF = 327680;
constexpr unsigned WS_HF = WS_OF + 2097152;
constexpr unsigned WS_DBG = 16u * 1024 * 1024;

#define STAMP(i)
#define CSTAMP(i)

DI unsigned pack_bf16(float lo, float hi) { f32x2 v = {lo, hi}; bf16x2_t r = __builtin_convertvector(v, bf16x2_t); return __builtin_bit_cast(unsigned, r); }
DI void glds(const char* sbase, unsigned voff, unsigned lds_base) {
    unsigned sv;
    asm volatile("s_mov_b32 %0, m0\n\ts_mov_b32 m0, %3\n\ts_nop 0\n\tglobal_load_lds_dwordx4 %1, %2\n\ts_mov_b32 m0, %0"
                 : "=&s"(sv) : "v"(voff), "s"(sbase), "s"(RFL(lds_base)) : "memory");
}
DI void glds_dev(const char* sbase, unsigned voff, unsigned lds_base) {
    unsigned sv;
    asm volatile("s_mov_b32 %0, m0\n\ts_mov_b32 m0, %3\n\ts_nop 0\n\tglobal_load_lds_dwordx4 %1, %2 sc1\n\ts_mov_b32 m0, %0"
                 : "=&s"(sv) : "v"(voff), "s"(sbase), "s"(RFL(lds_base)) : "memory");
}
DI u32x4 gld16_async(const char* sbase, unsigned voff) { u32x4 r; asm volatile("global_load_dwordx4 %0, %1, %2" : "=v"(r) : "v"(voff), "s"(sbase) : "memory"); return r; }
DI unsigned gld4_async(const char* sbase, unsigned voff) { unsigned r; asm volatile("global_load_dword %0, %1, %2" : "=v"(r) : "v"(voff), "s"(sbase) : "memory"); return r; }
DI void gst16_dev(char* sbase, unsigned voff, u32x4 v) { asm volatile("global_store_dwordx4 %0, %1, %2 sc1\n\ts_nop 1" :: "v"(voff), "v"(v), "s"(sbase) : "memory"); }
template <int N> DI void wait_vm_bar() { asm volatile("s_waitcnt vmcnt(%0) lgkmcnt(0)\n\ts_barrier" :: "i"(N) : "memory"); }
template <int N> DI void wait_vm_bar_lo() { asm volatile("s_setprio 0\n\ts_waitcnt vmcnt(%0) lgkmcnt(0)\n\ts_barrier\n\ts_setprio 3" :: "i"(N) : "memory"); }
DI void lds_bar_lo() { asm volatile("s_setprio 0\n\ts_waitcnt lgkmcnt(0)\n\ts_barrier\n\ts_setprio 3" ::: "memory"); }
DI void lds_bar() { asm volatile("s_waitcnt lgkmcnt(0)\n\ts_barrier" ::: "memory"); }
DI bf16x8 lds_b128(lds_cptr p) { return *(const __attribute__((address_space(3))) bf16x8*)p; }
DI f32x4 lds_f4(lds_cptr p) { return *(const __attribute__((address_space(3))) f32x4*)p; }
DI void lds_st_f4(lds_ptr p, f32x4 v) { *(__attribute__((address_space(3))) f32x4*)p = v; }
DI void lds_st_f(lds_ptr p, float v) { *(__attribute__((address_space(3))) float*)p = v; }
DI float lds_f(lds_cptr p) { return *(const __attribute__((address_space(3))) float*)p; }
DI s16x4 lds_tr(lds_cptr p) { return __builtin_bit_cast(s16x4, __builtin_amdgcn_ds_read_tr16_b64_v4i16((__attribute__((address_space(3))) v4i16_t*)p)); }
DI bf16x8 cat(s16x4 a, s16x4 b) { return __builtin_shufflevector(a, b, 0, 1, 2, 3, 4, 5, 6, 7); }
DI u32x4 pack8(const f32x16& x, int s, float sc) {
    u32x4 p;
    p[0] = pack_bf16(x[8 * s + 0] * sc, x[8 * s + 1] * sc); p[1] = pack_bf16(x[8 * s + 2] * sc, x[8 * s + 3] * sc);
    p[2] = pack_bf16(x[8 * s + 4] * sc, x[8 * s + 5] * sc); p[3] = pack_bf16(x[8 * s + 6] * sc, x[8 * s + 7] * sc);
    return p;
}
DI u32x4 pack44(f32x4 a, f32x4 b, float sc) { u32x4 p; p[0] = pack_bf16(a[0] * sc, a[1] * sc); p[1] = pack_bf16(a[2] * sc, a[3] * sc); p[2] = pack_bf16(b[0] * sc, b[1] * sc); p[3] = pack_bf16(b[2] * sc, b[3] * sc); return p; }

#define PRIO_HI() __builtin_amdgcn_s_setprio(3)
#define PRIO_LO() __builtin_amdgcn_s_setprio(0)
template <int SLP> DI void line_wait(char* line, int n, unsigned long long tag, int lane) {
    const unsigned off = (lane < n ? lane : 0) * 8;
#pragma unroll 1
    for (unsigned s = 0; s < SPIN_CAP; ++s) {
        unsigned long long v;
        asm volatile("global_load_dwordx2 %0, %1, %2 sc1\n\ts_setprio 0\n\ts_waitcnt vmcnt(0)\n\ts_setprio 3" : "=v"(v) : "v"(off), "s"(line) : "memory");
        if (__all(v == tag)) break;
        PRIO_LO(); __builtin_amdgcn_s_sleep(SLP); PRIO_HI();
    }
}

struct T64 { unsigned xs, ws; int xoff[2], woff[2][2]; };
template <int KTOT, bool XFRAG, bool PERMN>
DI T64 t64_setup(int tok0, int n0, int w, int lane) {
    T64 t;
    if (XFRAG) t.xs = (((tok0 >> 5) + (w >> 2)) * (KTOT / 16) + (w & 3)) * 1024 + lane * 16;
    else { int row = 8 * w + (lane >> 3); int ch = (lane & 7) ^ ((row >> 1) & 7); t.xs = (tok0 + row) * 1024 + ch * 16; }
    { int row = 8 * w + (lane >> 3); int ch = (lane & 7) ^ (((row >> 1) & 1) << 2); t.ws = row * 1024 + n0 * 2 + ch * 16; }
    const int ti = w & 1, ni = (w >> 1) & 1, kh = w >> 2, h = lane >> 5, r32 = lane & 31;
#pragma unroll
    for (int i = 0; i < 2; ++i) {
        const int ks = 2 * kh + i;
        if (XFRAG) t.xoff[i] = (ti * 4 + ks) * 1024 + lane * 16;
        else { int tok = ti * 32 + r32; t.xoff[i] = tok * 128 + (((2 * ks + h) ^ ((tok >> 1) & 7)) * 16); }
        const int g = (lane >> 4) & 1, q = (lane & 15) >> 2, p = lane & 3;
        const int pp = PERMN ? (((p & 1) << 1) | (p >> 1)) : p;
        const int col = ni * 32 + g * 16 + 4 * pp;
#pragma unroll
        for (int tt = 0; tt < 2; ++tt) {
            const int row = XFRAG ? (16 * ks + 8 * tt + 4 * h + q) : (16 * ks + 8 * h + 4 * tt + q);
            t.woff[i][tt] = row * 128 + (((col >> 3) ^ (((row >> 1) & 1) << 2)) * 16) + (col & 7) * 2;
        }
    }
    return t;
}
template <bool NOM = false> DI void t64_compute(const T64& t, lds_cptr b, f32x16& acc) {
#pragma unroll
    for (int i = 0; i < 2; ++i) {
        bf16x8 xf = lds_b128(b + t.xoff[i]);
        bf16x8 wf = cat(lds_tr(b + 8192 + t.woff[i][0]), lds_tr(b + 8192 + t.woff[i][1]));
        if (NOM) asm volatile("" :: "v"(wf), "v"(xf)); else acc = MFMA(wf, xf, acc);
    }
}
DI void t64_xchg_write(const f32x16& acc, lds_ptr scr, int w, int lane) {
    const int kh = w >> 2;
    f32x4 a = kh ? (f32x4){acc[0], acc[1], acc[2], acc[3]} : (f32x4){acc[8], acc[9], acc[10], acc[11]};
    f32x4 b = kh ? (f32x4){acc[4], acc[5], acc[6], acc[7]} : (f32x4){acc[12], acc[13], acc[14], acc[15]};
    lds_st_f4(scr + w * 2048 + lane * 16, a); lds_st_f4(scr + w * 2048 + 1024 + lane * 16, b);
}
DI void t64_xchg_read(const f32x16& acc, lds_cptr scr, int w, int lane, f32x4& r0, f32x4& r1) {
    const int kh = w >> 2;
    r0 = kh ? (f32x4){acc[8], acc[9], acc[10], acc[11]} : (f32x4){acc[0], acc[1], acc[2], acc[3]};
    r1 = kh ? (f32x4){acc[12], acc[13], acc[14], acc[15]} : (f32x4){acc[4], acc[5], acc[6], acc[7]};
    r0 += lds_f4(scr + (w ^ 4) * 2048 + lane * 16); r1 += lds_f4(scr + (w ^ 4) * 2048 + 1024 + lane * 16);
}

template <bool EXTRA, class F>
DI void kv_tile(int tile, const char* x, const char* wk, const char* wv, char* ws, char* lds, int w, int lane, unsigned long long tag, F extra) {
    __builtin_amdgcn_s_setprio(3);
    const int h = lane >> 5, r32 = lane & 31;
    const unsigned sb = (unsigned)(uintptr_t)lds + w * 8192;
    { const int row = lane >> 3; const unsigned xo = (tile * 32 + row) * 1024 + w * 128;
#pragma unroll
      for (int i = 0; i < 4; ++i) glds(x + i * 8192, xo + (((lane & 7) ^ ((i * 4 + (row >> 1)) & 7)) * 16), sb + i * 1024);
      const unsigned wo_ = w * 2048 + lane * 16;
      glds(wv, wo_, sb + 4096); glds(wv + 1024, wo_, sb + 5120); glds(wk, wo_, sb + 6144); glds(wk + 1024, wo_, sb + 7168); }
    STAMP(11);
    asm volatile("s_waitcnt vmcnt(0)" ::: "memory");
    extra();
    f32x16 a = {};
    const lds_cptr xb = (lds_cptr)lds + w * 8192 + r32 * 128;
    const lds_cptr wb = (lds_cptr)lds + w * 8192 + 4096 + ((lane >> 4) & 1) * 2048 + (8 * h + ((lane & 15) >> 2)) * 32 + (lane & 3) * 8;
#pragma unroll
    for (int ks = 0; ks < 4; ++ks) a = MFMA(cat(lds_tr(wb + ks * 512), lds_tr(wb + ks * 512 + 128)), lds_b128(xb + (((2 * ks + h) ^ ((r32 >> 1) & 7)) * 16)), a);
    STAMP(12);
    { lds_ptr p = (lds_ptr)lds + w * 8192;
      lds_st_f4(p + lane * 16, (f32x4){a[8], a[9], a[10], a[11]}); lds_st_f4(p + 1024 + lane * 16, (f32x4){a[12], a[13], a[14], a[15]});
#pragma unroll
      for (int r = 0; r < 8; ++r) lds_st_f(p + 2048 + ((r & 3) + 8 * (r >> 2) + 4 * h) * 144 + r32 * 4, a[r]); }
    lds_bar();
    if (w < 2) {
        f32x4 s0 = {}, s1 = {};
        const lds_cptr p = (lds_cptr)lds + (w == 0 ? lane * 16 : 2048 + (lane & 15) * 144 + ((lane >> 4) & 1) * 16 + h * 64);
        const int d1 = w == 0 ? 1024 : 32;
#pragma unroll
        for (int q = 0; q < 8; ++q) { s0 += lds_f4(p + q * 8192); s1 += lds_f4(p + q * 8192 + d1); }
        gst16_dev(ws + WS_KV + tile * 2048 + w * 1024, lane * 16, pack44(s0, s1, 1.0f));
        STAMP(13);
    }
    wait_vm_bar<0>();
    if (w == HSW && lane < NREP) __hip_atomic_store((gu64*)(ws + WS_KVF) + lane * 64 + tile, tag, RLX_AGENT);
    STAMP(14);
}

DI float bf_lo(unsigned u) { return __uint_as_float(u << 16); }
DI float bf_hi(unsigned u) { return __uint_as_float(u & 0xffff0000u); }
DI void gate_job(int tp, const char* x, const char* gw, char* ws, int w, int lane) {
    const u32x4 gv = *(const u32x4*)(gw + lane * 16);
    float res = 0.f;
#pragma unroll
    for (int t = 0; t < 8; ++t) {
        const u32x4 xv = *(const u32x4*)(x + (size_t)(tp * 64 + w * 8 + t) * 1024 + lane * 16);
        float s = 0.f;
#pragma unroll
        for (int i = 0; i < 4; ++i) s += bf_lo(xv[i]) * bf_lo(gv[i]) + bf_hi(xv[i]) * bf_hi(gv[i]);
#pragma unroll
        for (int o = 32; o >= 1; o >>= 1) s += __shfl_xor(s, o);
        if (lane == t) res = s;
    }
    if (lane < 8) __hip_atomic_store((gu32*)(ws + WS_G) + tp * 64 + w * 8 + lane, __float_as_uint(1.0f / (1.0f + __expf(-res))), RLX_AGENT);
    __builtin_amdgcn_s_waitcnt(0x0F70);
}

DI void up_job(int rp, int cp, const char* x, const char* wg, const char* wu, char* ws, char* lds, int w, int lane, unsigned long long tag) {
    constexpr int STB = 24576, NST = 16;
    const unsigned lds0 = (unsigned)(uintptr_t)lds + w * 1024;
    unsigned xs, gs;
    { int row = 16 * w + (lane >> 2); int c = (lane & 3) ^ ((row >> 2) & 3); xs = (rp * 128 + row) * 1024 + c * 16; }
    { int row = 4 * w + (lane >> 4); int c = (lane & 15) ^ ((row & 3) << 2); gs = row * (F_ * 2) + cp * 256 + c * 16; }
    const int rot = UPROT * (rp & 3);
#define UP_STAGE(s) do { const unsigned b_ = lds0 + ((s) % 3) * STB; const int k_ = ((s) + rot) & 15; glds(x + k_ * 64, xs, b_); glds(wg + k_ * (32 * F_ * 2), gs, b_ + 8192); glds(wu + k_ * (32 * F_ * 2), gs, b_ + 16384); } while (0)
    UP_STAGE(0); UP_STAGE(1);
    const int th = w & 1, fg = w >> 1, h = lane >> 5, r32 = lane & 31;
    lds_cptr xb[2], wb;
#pragma unroll
    for (int ks = 0; ks < 2; ++ks) { int tok = th * 64 + r32; xb[ks] = (lds_cptr)lds + tok * 64 + (((2 * ks + h) ^ ((tok >> 2) & 3)) * 16); }
    { const int g = (lane >> 4) & 1, q = (lane & 15) >> 2, p = lane & 3; const int col = fg * 32 + g * 16 + 4 * p;
      wb = (lds_cptr)lds + 8192 + (8 * h + q) * 256 + (((col >> 3) ^ (q << 2)) * 16) + (col & 7) * 2; }
    f32x16 ag[2] = {}, au[2] = {};
#pragma unroll
    for (int s = 0; s < NST; ++s) {
        if (s + 1 < NST) wait_vm_bar<3>(); else wait_vm_bar<0>();
        if (s + 2 < NST) UP_STAGE(s + 2);
        const int so = (s % 3) * STB;
        bf16x8 x0[2], x1[2], gf[2], uf[2];
#pragma unroll
        for (int ks = 0; ks < 2; ++ks) {
            if (GX & 1) { x0[ks] = x1[ks] = gf[ks] = uf[ks] = bf16x8{}; continue; }
            x0[ks] = lds_b128(xb[ks] + so); x1[ks] = lds_b128(xb[ks] + so + 2048);
            gf[ks] = cat(lds_tr(wb + so + ks * 4096), lds_tr(wb + so + ks * 4096 + 1024));
            uf[ks] = cat(lds_tr(wb + so + 8192 + ks * 4096), lds_tr(wb + so + 8192 + ks * 4096 + 1024));
        }
#pragma unroll
        for (int ks = 0; ks < 2; ++ks) {
            if (GX & 1) continue;
            if (GX & 4) { asm volatile("" :: "v"(x0[ks]), "v"(x1[ks]), "v"(gf[ks]), "v"(uf[ks])); continue; }
            ag[0] = MFMA(gf[ks], x0[ks], ag[0]); ag[1] = MFMA(gf[ks], x1[ks], ag[1]);
            au[0] = MFMA(uf[ks], x0[ks], au[0]); au[1] = MFMA(uf[ks], x1[ks], au[1]);
        }
    }
#undef UP_STAGE
    __builtin_amdgcn_s_setprio(3);
    STAMP(2);
#pragma unroll
    for (int i = 0; i < 2; ++i) {
        f32x16 hh = ag[i] * au[i];
        char* dst = ws + WS_HF + ((rp * 4 + th * 2 + i) * 112 + cp * 8 + fg * 2) * 1024;
        gst16_dev(dst, lane * 16, pack8(hh, 0, 1.0f));
        gst16_dev(dst, 1024 + lane * 16, pack8(hh, 1, 1.0f));
    }
    wait_vm_bar_lo<0>();
    if (w == HSW && lane < 16) __hip_atomic_store((gu64*)(ws + WS_HL + (rp * 16 + lane) * 128) + cp, tag, RLX_AGENT);
}

DI void down_out_job(int tp, int cg, int jw, const char* wd, const char* wo, char* ws, char* out, char* lds, int w, int lane, unsigned long long tag) {
    constexpr int NS = 5, NST = 28;
    const unsigned lds0 = (unsigned)(uintptr_t)lds + w * 1024;
    const T64 t = t64_setup<1792, true, true>(tp * 64, cg * 64, w, lane);
    const char* hf = ws + WS_HF;
    const int drot = DROT * ((7 * (tp & 3) + (7 * cg) / 2) % 28);
#define DK(s) ((s) + drot >= 28 ? (s) + drot - 28 : (s) + drot)
#pragma unroll
    for (int s = 0; s < 8; ++s) glds(wo + s * 65536, t.ws, lds0 + s * 8192);
    wait_vm_bar_lo<0>();
    bf16x8 wf[16];
#pragma unroll
    for (int s = 0; s < 8; ++s)
#pragma unroll
        for (int i = 0; i < 2; ++i) wf[2 * s + i] = cat(lds_tr((lds_cptr)lds + s * 8192 + t.woff[i][0]), lds_tr((lds_cptr)lds + s * 8192 + t.woff[i][1]));
    lds_bar_lo();
#pragma unroll
    for (int s = 0; s < 5; ++s) glds(wd + DK(s) * 65536, t.ws, lds0 + s * 16384 + 8192);
    if (w == HSW) line_wait<CNTSLEEP>(ws + WS_HL + (tp * 8 + cg) * 128, 14, tag, lane);
    wait_vm_bar_lo<0>();
    STAMP(3);
#pragma unroll
    for (int s = 0; s < 5; ++s) glds_dev(hf + DK(s) * 4096, t.xs, lds0 + s * 16384);
    f32x16 acc = {};
    __builtin_amdgcn_s_setprio(DPRIO);
#define D_STAGE(s) do { const unsigned b_ = lds0 + ((s) % NS) * 16384; const int k_ = DK(s); glds_dev(hf + k_ * 4096, t.xs, b_); glds(wd + k_ * 65536, t.ws, b_ + 8192); } while (0)
#pragma unroll
    for (int k = 0; k < NST / 2; ++k) {
        if (k == 0) wait_vm_bar<3>(); else if (k == 1) wait_vm_bar<1>(); else if (k < NST / 2 - 1) wait_vm_bar<2>(); else wait_vm_bar<0>();
        if (k >= 1) { if (2 * k + 3 < NST) D_STAGE(2 * k + 3); if (2 * k + 4 < NST) D_STAGE(2 * k + 4); }
        if (!(GX & 2)) {
        t64_compute<(GX & 4) != 0>(t, (lds_cptr)lds + ((2 * k) % NS) * 16384, acc);
        t64_compute<(GX & 4) != 0>(t, (lds_cptr)lds + ((2 * k + 1) % NS) * 16384, acc); }
    }
#undef D_STAGE
    __builtin_amdgcn_s_setprio(3);
    STAMP(4);
    if (w == HSW) line_wait<OSLEEP>(ws + WS_OL + (tp * 8 + cg) * 128, 9, tag, lane);
    lds_bar_lo();
    STAMP(5);
    const unsigned os = ((tp * 2 + (w >> 2)) * 32 + (w & 3)) * 1024 + lane * 16;
#pragma unroll
    for (int s = 0; s < 8; ++s) glds_dev(ws + WS_OF + s * 4096, os, lds0 + s * 8192);
    const int tok = tp * 64 + (w & 1) * 32 + (lane & 31);
    unsigned gu; asm volatile("global_load_dword %0, %1, %2 sc1" : "=v"(gu) : "v"(tok * 4), "s"(ws + WS_G) : "memory");
    f32x16 acca = {};
#pragma unroll
    for (int s = 0; s < 8; ++s) {
        if (s == 0) wait_vm_bar<5>(); else if (s == 4) wait_vm_bar<1>();
#pragma unroll
        for (int i = 0; i < 2; ++i) acca = MFMA(wf[2 * s + i], lds_b128((lds_cptr)lds + s * 8192 + t.xoff[i]), acca);
    }
    asm volatile("s_waitcnt vmcnt(0)" : "+v"(gu) :: "memory");
    const float g = __uint_as_float(gu), g1 = 1.0f - g;
#pragma unroll
    for (int i = 0; i < 16; ++i) acc[i] = g * acca[i] + g1 * acc[i];
    t64_xchg_write(acc, (lds_ptr)lds + 65536, w, lane);
    lds_bar();
    f32x4 r0, r1; t64_xchg_read(acc, (lds_cptr)lds + 65536, w, lane, r0, r1);
    *(u32x4*)(out + (size_t)tok * 1024 + (cg * 64 + ((w >> 1) & 1) * 32 + (w >> 2) * 16 + 8 * (lane >> 5)) * 2) = pack44(r0, r1, 1.0f);
}

constexpr int KVT = 2080;
DI float xhalf_max(float m) { auto rr = __builtin_amdgcn_permlane32_swap(__float_as_uint(m), __float_as_uint(m), false, false); return fmaxf(__uint_as_float(rr[0]), __uint_as_float(rr[1])); }
DI float max16(const f32x16& s) {
    float a = fmaxf(fmaxf(s[0], s[1]), s[2]), b = fmaxf(fmaxf(s[3], s[4]), s[5]);
    a = fmaxf(fmaxf(a, s[6]), s[7]); b = fmaxf(fmaxf(b, s[8]), s[9]); a = fmaxf(fmaxf(a, s[10]), s[11]); b = fmaxf(fmaxf(b, s[12]), s[13]);
    a = fmaxf(fmaxf(a, s[14]), s[15]); return fmaxf(a, b);
}
DI u32x4 exp_pack(const f32x16& s, int o) {
    u32x4 p;
#pragma unroll
    for (int i = 0; i < 4; ++i) p[i] = pack_bf16(__builtin_amdgcn_exp2f(s[o + 2 * i]), __builtin_amdgcn_exp2f(s[o + 2 * i + 1]));
    return p;
}
DI f32x16 attn_exact(const char* kv, bf16x8 qf, int lane) {
    f32x16 o = {}; float m = -INFINITY;
#pragma unroll 1
    for (int t = 0; t < 64; ++t) {
        u32x4 kf, v0, v1;
        asm volatile("global_load_dwordx4 %0, %3, %5 sc1\n\tglobal_load_dwordx4 %1, %4, %5 offset:1024 sc1\n\tglobal_load_dwordx4 %2, %4, %5 offset:1536 sc1\n\ts_waitcnt vmcnt(0)"
                     : "=&v"(kf), "=&v"(v0), "=&v"(v1) : "v"(t * 2048 + lane * 16), "v"(t * 2048 + (lane >> 5) * 256 + (lane & 15) * 16), "s"(kv) : "memory");
        if (lane & 16) { const unsigned c = (lane & 11) == 0 ? 0x3F803F80u : 0u; v0 = (u32x4){c, c, c, c}; v1 = v0; }
        f32x16 s = MFMA(__builtin_bit_cast(bf16x8, kf), qf, (f32x16){});
        const float mn = fmaxf(m, xhalf_max(max16(s)));
        const float al = __builtin_amdgcn_exp2f(m - mn);
        m = mn;
#pragma unroll
        for (int i = 0; i < 16; ++i) { o[i] *= al; s[i] -= mn; }
        o = MFMA(__builtin_bit_cast(bf16x8, v0), __builtin_bit_cast(bf16x8, exp_pack(s, 0)), o);
        o = MFMA(__builtin_bit_cast(bf16x8, v1), __builtin_bit_cast(bf16x8, exp_pack(s, 8)), o);
    }
    return o;
}
#define A_E(r) "v_exp_f32 v" #r ", v" #r "\n\t"
#define A_C(d, a, b) "v_cvt_pk_bf16_f32 v" #d ", v" #a ", v" #b "\n\t"
#define A_QK(d) "v_mfma_f32_32x32x16_bf16 v[" d "], v[100:103], v[96:99], v[80:95]\n\t"
#define A_PV(a, p) "v_mfma_f32_32x32x16_bf16 v[32:47], v[" a "], v[" p "], v[32:47]\n\t"
#define A_W "s_waitcnt lgkmcnt(2)\n\t"
#define A_RD(d, base, t) "ds_read_b128 v[" d "], " base " offset:((" t ")%%32)*2080\n\t"
#define A_T(S0,S1,S2,S3,S4,S5,S6,S7,S8,S9,S10,S11,S12,S13,S14,S15,P14,P15,SN, T) \
    A_E(S0) A_C(119,P14,P15) A_E(S1) A_E(S2) A_W A_PV("108:111","116:119") A_RD("108:111", "%10", T) A_E(S3) A_C(112,S0,S1) A_E(S4) A_E(S5) A_C(113,S2,S3) \
    A_W A_QK(SN) A_RD("100:103", "%8", T "+2") A_E(S6) A_E(S7) A_C(114,S4,S5) A_E(S8) A_E(S9) A_C(115,S6,S7) A_E(S10) A_E(S11) \
    A_W A_PV("104:107","112:115") A_RD("104:107", "%9", T "+1") A_C(116,S8,S9) A_E(S12) A_E(S13) A_C(117,S10,S11) A_E(S14) A_E(S15) A_C(118,S12,S13)
#define A_TA(T) A_T(48,49,50,51,52,53,54,55,56,57,58,59,60,61,62,63,78,79,"64:79",T)
#define A_TB(T) A_T(64,65,66,67,68,69,70,71,72,73,74,75,76,77,78,79,62,63,"48:63",T)
#define A_OPS "+{v[32:47]}"(o), "+{v[48:63]}"(sA), "+{v[64:79]}"(sB), "+{v[100:103]}"(kn), "+{v[104:107]}"(v0), "+{v[108:111]}"(v1), "+{v[112:115]}"(p0), "+{v[116:119]}"(p1)
#define A_CHUNK(C, BAR, G0, G1) \
    asm volatile(A_TA(#C "*8") A_TB(#C "*8+1") A_TA(#C "*8+2") BAR A_TB(#C "*8+3") G0 A_TA(#C "*8+4") G1 A_TB(#C "*8+5") A_TA(#C "*8+6") A_TB(#C "*8+7") \
                 : A_OPS : "v"(bk), "v"(bv0), "v"(bv1), "v"(kvo), "s"(kv + ((C) + 3) * 16384), "s"(dst0 + (((C) + 3) & 3) * 8 * KVT), "{v[80:95]}"(cn), "{v[96:99]}"(qf) : "memory")
#define A_BAR(n) "s_waitcnt vmcnt(" #n ")\n\ts_barrier\n\t"
#define A_G0 "s_mov_b32 m0, %13\n\ts_nop 0\n\tglobal_load_lds_dwordx4 %11, %12 sc1\n\t"
#define A_G1 "global_load_lds_dwordx4 %11, %12 offset:1024 sc1\n\t"
DI void attn_role(int blk, const char* x, const char* wq, const char* wk, const char* wv, char* ws, char* lds, int w, int lane, unsigned long long tag) {
    const int xcd = blk & 7, tp = xcd * 4 + ((blk >> 3) & 3), hg = blk >> 5;
    const unsigned lds0 = (unsigned)(uintptr_t)lds;
    const T64 t = t64_setup<512, false, false>(tp * 64, hg * 64, w, lane);
    const int qrot = QROT * (hg + 2 * (tp & 3));
#define Q_STAGE(s) do { const unsigned b_ = lds0 + w * 1024 + (((s) + 4) % 5) * 16384; const int k_ = ((s) + qrot) & 7; glds(x + k_ * 128, t.xs, b_); glds(wq + k_ * 65536, t.ws, b_ + 8192); } while (0)
#define Q_COMP(s) t64_compute(t, (lds_cptr)lds + (((s) + 4) % 5) * 16384, acc)
    if (hg >= KVHG) {
        kv_tile<true>(KVHG == 7 ? tp * 2 : tp * 2 + hg - 6, x, wk, wv, ws, lds, w, lane, tag, [&]() { Q_STAGE(0); });
        Q_STAGE(1); Q_STAGE(2); Q_STAGE(3); Q_STAGE(4);
        __builtin_amdgcn_s_setprio(0);
    } else {
        Q_STAGE(0); Q_STAGE(1); Q_STAGE(2); Q_STAGE(3); Q_STAGE(4);
    }
    f32x16 acc = {};
    STAMP(9);
    wait_vm_bar<8>(); STAMP(10);
    Q_COMP(0);
    wait_vm_bar<6>(); Q_STAGE(5); Q_COMP(1);
    wait_vm_bar<6>(); Q_STAGE(6); Q_COMP(2);
    wait_vm_bar<6>(); Q_STAGE(7); Q_COMP(3);
    wait_vm_bar<6>(); Q_COMP(4);
    wait_vm_bar<4>(); Q_COMP(5);
    wait_vm_bar<2>(); Q_COMP(6);
    wait_vm_bar<0>(); Q_COMP(7);
#undef Q_STAGE
#undef Q_COMP
    t64_xchg_write(acc, (lds_ptr)lds + 49152, w, lane);
    lds_bar();
    f32x4 r0, r1; t64_xchg_read(acc, (lds_cptr)lds + 49152, w, lane, r0, r1);
    const bf16x8 qf = __builtin_bit_cast(bf16x8, pack44(r0, r1, QSCALE));
    STAMP(1);
    if (w == HSW) line_wait<KVSLEEP>(ws + WS_KVF + (blk & (NREP - 1)) * 512, 64, tag, lane);
    PRIO_LO();
    lds_bar();
    STAMP(2);
    const char* kv = ws + WS_KV;
    const unsigned kvo = w * 2048 + lane * 16;
    const unsigned dst0 = RFL(lds0 + w * KVT);
#pragma unroll
    for (int c = 0; c < 4; ++c) { glds_dev(kv + c * 16384, kvo, dst0 + c * 8 * KVT); glds_dev(kv + c * 16384 + 1024, kvo, dst0 + c * 8 * KVT + 1024); }
    if (w == 1) { const unsigned c = (lane & 1) ? 0u : 0x3F803F80u; *(__attribute__((address_space(3))) u32x4*)((lds_ptr)lds + (lane >> 1) * KVT + 2048 + (lane & 1) * 16) = (u32x4){c, c, c, c}; }
    wait_vm_bar<6>();
    STAMP(3); CSTAMP(5);
    const unsigned bk = lds0 + lane * 16;
    const unsigned pad = lds0 + 2048 + ((lane & 11) == 0 ? 0 : 16);
    const unsigned bv0 = (lane & 16) ? pad : lds0 + 1024 + (lane >> 5) * 256 + (lane & 15) * 16;
    const unsigned bv1 = (lane & 16) ? pad : bv0 + 512;
    const lds_cptr lb = (lds_cptr)lds + lane * 16;
    f32x16 cn;
    f32x16 sA = MFMA(lds_b128(lb), qf, (f32x16){});
    { const float m = xhalf_max(max16(sA));
#pragma unroll
      for (int i = 0; i < 16; ++i) { cn[i] = -m; sA[i] -= m; } }
    f32x16 o = {}, sB = {};
    bf16x8 kn = lds_b128(lb + KVT);
    bf16x8 v0 = lds_b128((lds_cptr)(uintptr_t)bv0), v1 = v0;
    u32x4 p0 = {0u, 0u, 0u, 0u}, p1 = {0u, 0u, 0u, 0u};
    unsigned m0save; asm volatile("s_mov_b32 %0, m0\n\ts_nop 1" : "=s"(m0save), A_OPS);
    A_CHUNK(0, A_BAR(4), "", "");
    A_CHUNK(1, A_BAR(2), A_G0, A_G1);
    A_CHUNK(2, A_BAR(2), A_G0, A_G1);
    A_CHUNK(3, A_BAR(2), A_G0, A_G1);
    A_CHUNK(4, A_BAR(2), A_G0, A_G1);
    A_CHUNK(5, A_BAR(2), "", "");
    A_CHUNK(6, A_BAR(0), "", "");
    A_CHUNK(7, "", "", "");
    asm volatile("s_mov_b32 m0, %0" :: "s"(m0save));
    asm volatile("s_nop 0\n\t" A_C(119,78,79) "s_waitcnt lgkmcnt(0)\n\ts_nop 1\n\t" A_PV("108:111","116:119") "s_nop 15" : A_OPS :: "memory");
    STAMP(4); CSTAMP(6);
    float mx = fabsf(o[0]);
#pragma unroll
    for (int i = 1; i < 8; ++i) mx = fmaxf(mx, fabsf(o[i]));
    const bool ok = (o[8] < 1.0e18f) && (o[8] > 0.5f) && (mx < 1.0e36f);
    if (__builtin_expect(!__all(ok), 0)) o = attn_exact(kv, qf, lane);
    const float inv = 1.0f / o[8];
    gst16_dev(ws + WS_OF + ((tp * 2 + (w & 1)) * 32 + hg * 4 + ((w >> 1) & 1) * 2 + (w >> 2)) * 1024, lane * 16, pack8(o, 0, inv));
    wait_vm_bar<0>();
    if (w == HSW && lane < 8) __hip_atomic_store((gu64*)(ws + WS_OL + (tp * 8 + lane) * 128) + hg, tag, RLX_AGENT);
}

__global__ __launch_bounds__(512, 4) void k_all(const char* __restrict__ x, const char* __restrict__ wq, const char* __restrict__ wk, const char* __restrict__ wv, const char* __restrict__ wo,
                                                const char* __restrict__ wg, const char* __restrict__ wu, const char* __restrict__ wd, const char* __restrict__ gw,
                                                char* __restrict__ ws, char* __restrict__ out) {
    extern __shared__ __attribute__((aligned(16))) char lds[];
    const int tid = threadIdx.x, lane = tid & 63; const int w = RFL(tid >> 6);
    const int b = blockIdx.x;
    STAMP(0);
    const unsigned long long tag = (llvm_dispatch_id() + 1) * 0x9E3779B97F4A7C15ull + (unsigned long long)__builtin_amdgcn_dispatch_ptr();
    if (b < 256) {
        attn_role(b, x, wq, wk, wv, ws, lds, w, lane, tag);
    } else {
        const int g = b - 256, xcd = g & 7, i = g >> 3;
        if (i < 28) up_job(4 * (xcd >> 1) + i / 7, 7 * (xcd & 1) + i % 7, x, wg, wu, ws, lds, w, lane, tag);
        else {
            const int tk = 4 * xcd + (i - 28);
            if (KVHG == 7) kv_tile<false>(tk * 2 + 1, x, wk, wv, ws, lds, w, lane, tag, []() {});
            PRIO_LO();
            __builtin_amdgcn_s_sleep(40);
            gate_job(tk, x, gw, ws, w, lane);
            wait_vm_bar_lo<0>();
            if (w == HSW && lane < 8) __hip_atomic_store((gu64*)(ws + WS_OL + (tk * 8 + lane) * 128) + 8, tag, RLX_AGENT);
        }
        STAMP(1);
        down_out_job(4 * xcd + (i >> 3), i & 7, i * 8 + w, wd, wo, ws, out, lds, w, lane, tag);
    }
    STAMP(7);
}

extern "C" void kernel_launch(void* const* d_in, const int* in_sizes, int n_in, void* d_out, int out_size, void* d_ws, size_t ws_size, hipStream_t stream) {
    (void)hipFuncSetAttribute((const void*)k_all, hipFuncAttributeMaxDynamicSharedMemorySize, 81920);
    k_all<<<512, 512, 81920, stream>>>((const char*)d_in[0], (const char*)d_in[1], (const char*)d_in[2], (const char*)d_in[3], (const char*)d_in[4],
                                       (const char*)d_in[6], (const char*)d_in[7], (const char*)d_in[8], (const char*)d_in[9], (char*)d_ws, (char*)d_out);
}
```

```cpp
#include <hip/hip_runtime.h>
#include <hip/hip_bf16.h>
#include <stdint.h>

typedef __attribute__((ext_vector_type(8))) short bf16x8;
typedef __attribute__((ext_vector_type(4))) short s16x4;
typedef __attribute__((ext_vector_type(16))) float f32x16;
typedef __attribute__((ext_vector_type(4))) float f32x4;
typedef __attribute__((ext_vector_type(4))) unsigned u32x4;
typedef __attribute__((ext_vector_type(2))) float f32x2;
typedef __attribute__((ext_vector_type(2))) __bf16 bf16x2_t;
typedef __attribute__((address_space(3))) const char* lds_cptr;
typedef __attribute__((address_space(3))) char* lds_ptr;
typedef __attribute__((address_space(1))) unsigned gu32;
typedef __attribute__((address_space(1))) unsigned long long gu64;
typedef short v4i16_t __attribute__((ext_vector_type(4)));
extern "C" __device__ unsigned long long llvm_dispatch_id() __asm("llvm.amdgcn.dispatch.id");
#define DI __device__ __forceinline__
#define MFMA(a, b, c) __builtin_amdgcn_mfma_f32_32x32x16_bf16((a), (b), (c), 0, 0, 0)
#define UMFMA(a, b, c) ((GX & 16) ? keep2(a, b, c) : MFMA(a, b, c))
#define RLX_AGENT __ATOMIC_RELAXED, __HIP_MEMORY_SCOPE_AGENT
#define RFL(x) __builtin_amdgcn_readfirstlane(x)

#define KVSLEEP 6
#define CNTSLEEP 10
constexpr int HSW = 0;
#define NREP 8
#define OSLEEP 4
#define DPRIO 0
#define KVHG 7
#define UPROT 4
#define UPDEEP 0
#define QROT 1
#define SPLITBAR 0
#define GX 0
#define TCH2 1
#define DROT 1
#define HPLAIN 0
#define KDELAY 40
constexpr int F_ = 1792;
constexpr float QSCALE = 0.25f * 1.4426950408889634f;
constexpr unsigned SPIN_CAP = 1u << 20;

constexpr unsigned WS_KVF = 0;
constexpr unsigned WS_G  = 4096;
constexpr unsigned WS_HL = 16384;
constexpr unsigned WS_OL = WS_HL + 32768;
constexpr unsigned WS_KV = 131072;
constexpr unsigned WS_OF = 327680;
constexpr unsigned WS_HF = WS_OF + 2097152;
constexpr unsigned WS_DBG = 16u * 1024 * 1024;

#define STAMP(i)
#define CSTAMP(i)

DI f32x16 keep2(bf16x8 a, bf16x8 b, f32x16 c) { asm volatile("" :: "v"(a), "v"(b)); return c; }
DI unsigned pack_bf16(float lo, float hi) { f32x2 v = {lo, hi}; bf16x2_t r = __builtin_convertvector(v, bf16x2_t); return __builtin_bit_cast(unsigned, r); }
DI void glds(const char* sbase, unsigned voff, unsigned lds_base) {
    unsigned sv;
    asm volatile("s_mov_b32 %0, m0\n\ts_mov_b32 m0, %3\n\ts_nop 0\n\tglobal_load_lds_dwordx4 %1, %2\n\ts_mov_b32 m0, %0"
                 : "=&s"(sv) : "v"(voff), "s"(sbase), "s"(RFL(lds_base)) : "memory");
}
DI void glds_dev(const char* sbase, unsigned voff, unsigned lds_base) {
    unsigned sv;
    asm volatile("s_mov_b32 %0, m0\n\ts_mov_b32 m0, %3\n\ts_nop 0\n\tglobal_load_lds_dwordx4 %1, %2 sc1\n\ts_mov_b32 m0, %0"
                 : "=&s"(sv) : "v"(voff), "s"(sbase), "s"(RFL(lds_base)) : "memory");
}
DI u32x4 gld16_async(const char* sbase, unsigned voff) { u32x4 r; asm volatile("global_load_dwordx4 %0, %1, %2" : "=v"(r) : "v"(voff), "s"(sbase) : "memory"); return r; }
DI unsigned gld4_async(const char* sbase, unsigned voff) { unsigned r; asm volatile("global_load_dword %0, %1, %2" : "=v"(r) : "v"(voff), "s"(sbase) : "memory"); return r; }
DI void gst16_dev(char* sbase, unsigned voff, u32x4 v) { asm volatile("global_store_dwordx4 %0, %1, %2 sc1\n\ts_nop 1" :: "v"(voff), "v"(v), "s"(sbase) : "memory"); }
template <int N> DI void wait_vm_bar() { asm volatile("s_waitcnt vmcnt(%0) lgkmcnt(0)\n\ts_barrier" :: "i"(N) : "memory"); }
template <int N> DI void wait_vm_bar_lo() { asm volatile("s_setprio 0\n\ts_waitcnt vmcnt(%0) lgkmcnt(0)\n\ts_barrier\n\ts_setprio 3" :: "i"(N) : "memory"); }
DI void lds_bar_lo() { asm volatile("s_setprio 0\n\ts_waitcnt lgkmcnt(0)\n\ts_barrier\n\ts_setprio 3" ::: "memory"); }
DI void lds_bar() { asm volatile("s_waitcnt lgkmcnt(0)\n\ts_barrier" ::: "memory"); }
DI bf16x8 lds_b128(lds_cptr p) { return *(const __attribute__((address_space(3))) bf16x8*)p; }
DI f32x4 lds_f4(lds_cptr p) { return *(const __attribute__((address_space(3))) f32x4*)p; }
DI void lds_st_f4(lds_ptr p, f32x4 v) { *(__attribute__((address_space(3))) f32x4*)p = v; }
DI void lds_st_f(lds_ptr p, float v) { *(__attribute__((address_space(3))) float*)p = v; }
DI float lds_f(lds_cptr p) { return *(const __attribute__((address_space(3))) float*)p; }
DI s16x4 lds_tr(lds_cptr p) { return __builtin_bit_cast(s16x4, __builtin_amdgcn_ds_read_tr16_b64_v4i16((__attribute__((address_space(3))) v4i16_t*)p)); }
DI bf16x8 cat(s16x4 a, s16x4 b) { return __builtin_shufflevector(a, b, 0, 1, 2, 3, 4, 5, 6, 7); }
DI u32x4 pack8(const f32x16& x, int s, float sc) {
    u32x4 p;
    p[0] = pack_bf16(x[8 * s + 0] * sc, x[8 * s + 1] * sc); p[1] = pack_bf16(x[8 * s + 2] * sc, x[8 * s + 3] * sc);
    p[2] = pack_bf16(x[8 * s + 4] * sc, x[8 * s + 5] * sc); p[3] = pack_bf16(x[8 * s + 6] * sc, x[8 * s + 7] * sc);
    return p;
}
DI u32x4 pack44(f32x4 a, f32x4 b, float sc) { u32x4 p; p[0] = pack_bf16(a[0] * sc, a[1] * sc); p[1] = pack_bf16(a[2] * sc, a[3] * sc); p[2] = pack_bf16(b[0] * sc, b[1] * sc); p[3] = pack_bf16(b[2] * sc, b[3] * sc); return p; }

#define PRIO_HI() __builtin_amdgcn_s_setprio(3)
#define PRIO_LO() __builtin_amdgcn_s_setprio(0)
template <int SLP> DI void line_wait(char* line, int n, unsigned long long tag, int lane) {
    const unsigned off = (lane < n ? lane : 0) * 8;
#pragma unroll 1
    for (unsigned s = 0; s < SPIN_CAP; ++s) {
        unsigned long long v;
        asm volatile("global_load_dwordx2 %0, %1, %2 sc1\n\ts_setprio 0\n\ts_waitcnt vmcnt(0)\n\ts_setprio 3" : "=v"(v) : "v"(off), "s"(line) : "memory");
        if (__all(v == tag)) break;
        PRIO_LO(); __builtin_amdgcn_s_sleep(SLP); PRIO_HI();
    }
}

struct T64 { unsigned xs, ws; int xoff[2], woff[2][2]; };
template <int KTOT, bool XFRAG, bool PERMN>
DI T64 t64_setup(int tok0, int n0, int w, int lane) {
    T64 t;
    if (XFRAG) t.xs = (((tok0 >> 5) + (w >> 2)) * (KTOT / 16) + (w & 3)) * 1024 + lane * 16;
    else { int row = 8 * w + (lane >> 3); int ch = (lane & 7) ^ ((row >> 1) & 7); t.xs = (tok0 + row) * 1024 + ch * 16; }
    { int row = 8 * w + (lane >> 3); int ch = (lane & 7) ^ (((row >> 1) & 1) << 2); t.ws = row * 1024 + n0 * 2 + ch * 16; }
    const int ti = w & 1, ni = (w >> 1) & 1, kh = w >> 2, h = lane >> 5, r32 = lane & 31;
#pragma unroll
    for (int i = 0; i < 2; ++i) {
        const int ks = 2 * kh + i;
        if (XFRAG) t.xoff[i] = (ti * 4 + ks) * 1024 + lane * 16;
        else { int tok = ti * 32 + r32; t.xoff[i] = tok * 128 + (((2 * ks + h) ^ ((tok >> 1) & 7)) * 16); }
        const int g = (lane >> 4) & 1, q = (lane & 15) >> 2, p = lane & 3;
        const int pp = PERMN ? (((p & 1) << 1) | (p >> 1)) : p;
        const int col = ni * 32 + g * 16 + 4 * pp;
#pragma unroll
        for (int tt = 0; tt < 2; ++tt) {
            const int row = XFRAG ? (16 * ks + 8 * tt + 4 * h + q) : (16 * ks + 8 * h + 4 * tt + q);
            t.woff[i][tt] = row * 128 + (((col >> 3) ^ (((row >> 1) & 1) << 2)) * 16) + (col & 7) * 2;
        }
    }
    return t;
}
template <bool NOM = false> DI void t64_compute(const T64& t, lds_cptr b, f32x16& acc) {
#pragma unroll
    for (int i = 0; i < 2; ++i) {
        bf16x8 xf = lds_b128(b + t.xoff[i]);
        bf16x8 wf = cat(lds_tr(b + 8192 + t.woff[i][0]), lds_tr(b + 8192 + t.woff[i][1]));
        if (NOM) asm volatile("" :: "v"(wf), "v"(xf)); else acc = MFMA(wf, xf, acc);
    }
}
DI void t64_xchg_write(const f32x16& acc, lds_ptr scr, int w, int lane) {
    const int kh = w >> 2;
    f32x4 a = kh ? (f32x4){acc[0], acc[1], acc[2], acc[3]} : (f32x4){acc[8], acc[9], acc[10], acc[11]};
    f32x4 b = kh ? (f32x4){acc[4], acc[5], acc[6], acc[7]} : (f32x4){acc[12], acc[13], acc[14], acc[15]};
    lds_st_f4(scr + w * 2048 + lane * 16, a); lds_st_f4(scr + w * 2048 + 1024 + lane * 16, b);
}
DI void t64_xchg_read(const f32x16& acc, lds_cptr scr, int w, int lane, f32x4& r0, f32x4& r1) {
    const int kh = w >> 2;
    r0 = kh ? (f32x4){acc[8], acc[9], acc[10], acc[11]} : (f32x4){acc[0], acc[1], acc[2], acc[3]};
    r1 = kh ? (f32x4){acc[12], acc[13], acc[14], acc[15]} : (f32x4){acc[4], acc[5], acc[6], acc[7]};
    r0 += lds_f4(scr + (w ^ 4) * 2048 + lane * 16); r1 += lds_f4(scr + (w ^ 4) * 2048 + 1024 + lane * 16);
}

DI void kv_job(int tile, const char* x, const char* wk, const char* wv, const char* gw, char* ws, char* lds, int w, int lane, unsigned long long tag) {
    PRIO_HI();
    constexpr int WB = 8192;
    const int h = lane >> 5, r32 = lane & 31;
    const unsigned sb = (unsigned)(uintptr_t)lds + w * WB;
    { const int row = lane >> 3; const unsigned xo = (tile * 32 + row) * 1024 + w * 128;
      const unsigned xe = xo + (((lane & 7) ^ (row >> 1)) * 16), xd = xo + (((lane & 7) ^ (4 + (row >> 1))) * 16);
      const unsigned wo_ = w * 2048 + lane * 16;
#pragma unroll
      for (int i = 0; i < 4; ++i) glds(x + i * 8192, (i & 1) ? xd : xe, sb + i * 1024);
      glds(wv, wo_, sb + 4096); glds(wv + 1024, wo_, sb + 5120); glds(wk, wo_, sb + 6144); glds(wk + 1024, wo_, sb + 7168);
      glds(gw, lane * 16, (unsigned)(uintptr_t)lds + 65536 + w * 1024); }
    STAMP(11);
    asm volatile("s_waitcnt vmcnt(0)" ::: "memory");
    f32x16 a = {}, ga = {};
    const lds_cptr xb = (lds_cptr)lds + w * WB + r32 * 128;
    const lds_cptr wb = (lds_cptr)lds + w * WB + 4096 + ((lane >> 4) & 1) * 2048 + (8 * h + ((lane & 15) >> 2)) * 32 + (lane & 3) * 8;
    const lds_cptr gb = (lds_cptr)lds + 65536 + w * 1152 + h * 16;
#pragma unroll
    for (int ks = 0; ks < 4; ++ks) {
        const bf16x8 xf = lds_b128(xb + (((2 * ks + h) ^ ((r32 >> 1) & 7)) * 16));
        a = MFMA(cat(lds_tr(wb + ks * 512), lds_tr(wb + ks * 512 + 128)), xf, a); ga = MFMA(lds_b128(gb + ks * 32), xf, ga);
    }
    STAMP(12);
    { lds_ptr p = (lds_ptr)lds + w * WB;
      lds_st_f4(p + lane * 16, (f32x4){a[8], a[9], a[10], a[11]}); lds_st_f4(p + 1024 + lane * 16, (f32x4){a[12], a[13], a[14], a[15]});
#pragma unroll
      for (int r = 0; r < 8; ++r) lds_st_f(p + 2048 + ((r & 3) + 8 * (r >> 2) + 4 * h) * 144 + r32 * 4, a[r]);
      lds_st_f(p + 4352 + lane * 4, ga[0]); }
    lds_bar();
    if (w < 2) {
        f32x4 s0 = {}, s1 = {};
        const lds_cptr p = (lds_cptr)lds + (w == 0 ? lane * 16 : 2048 + (lane & 15) * 144 + ((lane >> 4) & 1) * 16 + h * 64);
        const int d1 = w == 0 ? 1024 : 32;
#pragma unroll
        for (int q = 0; q < 8; ++q) { s0 += lds_f4(p + q * WB); s1 += lds_f4(p + q * WB + d1); }
        gst16_dev(ws + WS_KV + tile * 2048 + w * 1024, lane * 16, pack44(s0, s1, 1.0f));
        STAMP(13);
    } else if (w == 2) {
        float s = 0.f;
#pragma unroll
        for (int q = 0; q < 8; ++q) s += lds_f((lds_cptr)lds + q * WB + 4352 + lane * 4);
        if (lane < 32) __hip_atomic_store((gu32*)(ws + WS_G) + tile * 32 + lane, __float_as_uint(1.0f / (1.0f + __expf(-s))), RLX_AGENT);
    }
    wait_vm_bar<0>();
    if (w == HSW && lane < NREP) __hip_atomic_store((gu64*)(ws + WS_KVF) + lane * 64 + tile, tag, RLX_AGENT);
    STAMP(14);
    PRIO_LO();
}

DI void up_job(int rp, int cp, const char* x, const char* wg, const char* wu, char* ws, char* lds, int w, int lane, unsigned long long tag) {
    constexpr int STB = 24576, NST = 16;
    const unsigned lds0 = (unsigned)(uintptr_t)lds + w * 1024;
    unsigned xs, gs;
    { int row = 16 * w + (lane >> 2); int c = (lane & 3) ^ ((row >> 2) & 3); xs = (rp * 128 + row) * 1024 + c * 16; }
    { int row = 4 * w + (lane >> 4); int c = (lane & 15) ^ ((row & 3) << 2); gs = row * (F_ * 2) + cp * 256 + c * 16; }
    int rot = UPROT * (rp & 3);
#define UP_STAGE(s) do { const unsigned b_ = lds0 + ((s) % 3) * STB; const int k_ = ((s) + rot) & 15; glds(x + k_ * 64, xs, b_); glds(wg + k_ * (32 * F_ * 2), gs, b_ + 8192); glds(wu + k_ * (32 * F_ * 2), gs, b_ + 16384); } while (0)
    UP_STAGE(0); UP_STAGE(1);
    const int th = w & 1, fg = w >> 1, h = lane >> 5, r32 = lane & 31;
    lds_cptr xb[2], wb;
#pragma unroll
    for (int ks = 0; ks < 2; ++ks) { int tok = th * 64 + r32; xb[ks] = (lds_cptr)lds + tok * 64 + (((2 * ks + h) ^ ((tok >> 2) & 3)) * 16); }
    { const int g = (lane >> 4) & 1, q = (lane & 15) >> 2, p = lane & 3; const int col = fg * 32 + g * 16 + 4 * p;
      wb = (lds_cptr)lds + 8192 + (8 * h + q) * 256 + (((col >> 3) ^ (q << 2)) * 16) + (col & 7) * 2; }
    f32x16 ag[2] = {}, au[2] = {};
    unsigned g0, g1, goff = (rp * 128 + th * 64 + r32) * 4;
    asm volatile("" : "+v"(goff));
#define SBAR() __builtin_amdgcn_sched_barrier(0)
#define UP_RD(F, so, ks) do { F[0] = cat(lds_tr(wb + (so) + (ks) * 4096), lds_tr(wb + (so) + (ks) * 4096 + 1024)); F[1] = lds_b128(xb[ks] + (so)); F[2] = lds_b128(xb[ks] + (so) + 2048); \
        F[3] = cat(lds_tr(wb + (so) + 8192 + (ks) * 4096), lds_tr(wb + (so) + 8192 + (ks) * 4096 + 1024)); } while (0)
    bf16x8 fa[4], fb[4];
#pragma unroll
    for (int s = 0; s < NST; ++s) {
        if (s + 1 < NST) wait_vm_bar<3>(); else wait_vm_bar<0>();
        const int so = (s % 3) * STB;
        SBAR(); UP_RD(fb, so, 0); SBAR();
        asm volatile("" : "+s"(rot));
        const unsigned b_ = lds0 + ((s + 2) % 3) * STB; const int k_ = (s + 2 + rot) & 15;
        if (s > 0) { ag[0] = UMFMA(fa[0], fa[1], ag[0]); asm volatile("" : "+v"(ag[0])); }
        SBAR(); if (s + 2 < NST && !(GX & 8)) glds(x + k_ * 64, xs, b_); SBAR();
        if (s > 0) { ag[1] = UMFMA(fa[0], fa[2], ag[1]); asm volatile("" : "+v"(ag[1])); }
        SBAR(); if (s + 2 < NST && !(GX & 8)) glds(wg + k_ * (32 * F_ * 2), gs, b_ + 8192); SBAR();
        if (s > 0) { au[0] = UMFMA(fa[3], fa[1], au[0]); asm volatile("" : "+v"(au[0])); }
        SBAR(); if (s + 2 < NST && !(GX & 8)) glds(wu + k_ * (32 * F_ * 2), gs, b_ + 16384); SBAR();
        if (s > 0) { au[1] = UMFMA(fa[3], fa[2], au[1]); asm volatile("" : "+v"(au[1])); }
        SBAR();
        if (s == NST - 3 && w == HSW) { PRIO_HI(); line_wait<CNTSLEEP>(ws + WS_KVF + (blockIdx.x & (NREP - 1)) * 512 + rp * 32, 4, tag, lane); PRIO_LO(); }
        if (s == NST - 2) asm volatile("global_load_dword %0, %2, %3 sc1\n\tglobal_load_dword %1, %2, %3 offset:128 sc1" : "=&v"(g0), "=&v"(g1) : "v"(goff), "s"(ws + WS_G) : "memory");
        UP_RD(fa, so, 1); SBAR();
        ag[0] = UMFMA(fb[0], fb[1], ag[0]); ag[1] = UMFMA(fb[0], fb[2], ag[1]); au[0] = UMFMA(fb[3], fb[1], au[0]); au[1] = UMFMA(fb[3], fb[2], au[1]);
        asm volatile("" : "+v"(ag[0]), "+v"(ag[1]), "+v"(au[0]), "+v"(au[1])); SBAR();
    }
    ag[0] = UMFMA(fa[0], fa[1], ag[0]); ag[1] = UMFMA(fa[0], fa[2], ag[1]); au[0] = UMFMA(fa[3], fa[1], au[0]); au[1] = UMFMA(fa[3], fa[2], au[1]);
#undef UP_RD
#undef UP_STAGE
    PRIO_HI();
    STAMP(2);
    asm volatile("" : "+v"(g0), "+v"(g1));
#pragma unroll
    for (int i = 0; i < 2; ++i) {
        const float sc = 1.0f - __uint_as_float(i ? g1 : g0);
        f32x16 hh = ag[i] * au[i];
        char* dst = ws + WS_HF + ((rp * 4 + th * 2 + i) * 112 + cp * 8 + fg * 2) * 1024;
        gst16_dev(dst, lane * 16, pack8(hh, 0, sc));
        gst16_dev(dst, 1024 + lane * 16, pack8(hh, 1, sc));
    }
    wait_vm_bar_lo<0>();
    if (w == HSW && lane < 16) __hip_atomic_store((gu64*)(ws + WS_HL + (rp * 16 + lane) * 128) + cp, tag, RLX_AGENT);
}

DI u32x4 gld16_dev(const char* sbase, unsigned voff) { u32x4 r; asm volatile("global_load_dwordx4 %0, %1, %2 sc1" : "=v"(r) : "v"(voff), "s"(sbase) : "memory"); return r; }
DI unsigned flag_mask(const char* line, unsigned off, unsigned long long tag) {
    unsigned long long v;
    asm volatile("global_load_dwordx2 %0, %1, %2 sc1\n\ts_waitcnt vmcnt(0)\n\ts_setprio 3" : "=v"(v) : "v"(off), "s"(line) : "memory");
    const unsigned m = (unsigned)__ballot(v == tag);
    PRIO_LO();
    return m;
}
DI void down_out_job(int tp, int cg, const char* wd, const char* wo, char* ws, char* out, char* lds, int w, int lane, unsigned long long tag) {
    constexpr int D = 3;
    const unsigned lds0 = (unsigned)(uintptr_t)lds;
    const int h = lane >> 5;
    const unsigned l16 = lane * 16;
    unsigned wsrc; { const int row = lane >> 3; wsrc = row * 1024 + cg * 128 + (((lane & 7) ^ (((row >> 1) & 1) << 2)) * 16); }
    int woff[2];
    { const int g = (lane >> 4) & 1, q = (lane & 15) >> 2, p = lane & 3, pp = ((p & 1) << 1) | (p >> 1), row = 4 * h + q;
#pragma unroll
      for (int ni = 0; ni < 2; ++ni) { const int col = ni * 32 + g * 16 + 4 * pp; woff[ni] = row * 128 + (((col >> 3) ^ (((row >> 1) & 1) << 2)) * 16) + (col & 7) * 2; } }
    const char* hx = ws + WS_HF + (tp * 224 + w) * 1024;
    const char* wdw = wd + w * 16384;
    const char* hl = ws + WS_HL + (tp * 8 + cg) * 128;
    const unsigned ring = RFL(lds0 + w * 8192);
    const lds_cptr ringp = (lds_cptr)lds + w * 8192;
    const int rot = (cg * 7 / 4 + (tp & 3) * 7 / 2) % 14;
    f32x16 acc[4] = {};
    u32x4 xr[D][2];
    asm volatile("" : "+v"(acc[0]), "+v"(acc[1]), "+v"(acc[2]), "+v"(acc[3]));
    if (w == HSW) {
        line_wait<CNTSLEEP>((char*)hl, 14, tag, lane);
        STAMP(12);
    }
    wait_vm_bar_lo<0>();
    STAMP(3);
    PRIO_LO();
#define HLD gld16_dev
#define DN_ISSUE(i) do { \
        const int p_ = (i) + rot >= 14 ? (i) + rot - 14 : (i) + rot; \
        xr[(i) % D][0] = HLD(hx + p_ * 8192, l16); xr[(i) % D][1] = HLD(hx + p_ * 8192 + 114688, l16); \
        glds(wdw + p_ * 131072, wsrc, ring + ((i) & 3) * 2048); glds(wdw + p_ * 131072 + 8192, wsrc, ring + ((i) & 3) * 2048 + 1024); } while (0)
    DN_ISSUE(0); DN_ISSUE(1); DN_ISSUE(2);
#pragma unroll
    for (int i = 0; i < 14; ++i) {
        if (i < 12) asm volatile("s_waitcnt vmcnt(8)" : "+v"(xr[i % D][0]), "+v"(xr[i % D][1]) :: "memory");
        else if (i == 12) asm volatile("s_waitcnt vmcnt(6)" : "+v"(xr[i % D][0]), "+v"(xr[i % D][1]) :: "memory");
        else asm volatile("s_waitcnt vmcnt(4)" : "+v"(xr[i % D][0]), "+v"(xr[i % D][1]) :: "memory");
        const lds_cptr rb = ringp + (i & 3) * 2048;
        const bf16x8 w0 = cat(lds_tr(rb + woff[0]), lds_tr(rb + woff[0] + 1024)), w1 = cat(lds_tr(rb + woff[1]), lds_tr(rb + woff[1] + 1024));
        const bf16x8 x0 = __builtin_bit_cast(bf16x8, xr[i % D][0]), x1 = __builtin_bit_cast(bf16x8, xr[i % D][1]);
        if (!(GX & 2)) { acc[0] = MFMA(w0, x0, acc[0]); acc[1] = MFMA(w0, x1, acc[1]); acc[2] = MFMA(w1, x0, acc[2]); acc[3] = MFMA(w1, x1, acc[3]); }
        asm volatile("" : "+v"(acc[0]), "+v"(acc[1]), "+v"(acc[2]), "+v"(acc[3]));
        if (i + D < 14) DN_ISSUE(i + D);
        else { const int j = i + D - 14; glds(wo + (w * 4 + j) * 16384, wsrc, ring + ((i + D) & 3) * 2048); glds(wo + (w * 4 + j) * 16384 + 8192, wsrc, ring + ((i + D) & 3) * 2048 + 1024); }
    }
    glds(wo + (w * 4 + 3) * 16384, wsrc, ring + (17 & 3) * 2048); glds(wo + (w * 4 + 3) * 16384 + 8192, wsrc, ring + (17 & 3) * 2048 + 1024);
#undef DN_ISSUE
    STAMP(4);
    PRIO_HI();
    STAMP(6);
    line_wait<OSLEEP>(ws + WS_OL + (tp * 8 + cg) * 128 + w * 8, 1, tag, lane);
    STAMP(5);
    const char* ox = ws + WS_OF + (tp * 64 + w * 4) * 1024;
    u32x4 of[4][2];
#pragma unroll
    for (int i = 0; i < 4; ++i) { of[i][0] = gld16_dev(ox + i * 1024, l16); of[i][1] = gld16_dev(ox + i * 1024 + 32768, l16); }
#pragma unroll
    for (int i = 0; i < 4; ++i) {
        const lds_cptr rb = ringp + ((i + 14) & 3) * 2048;
        const bf16x8 w0 = cat(lds_tr(rb + woff[0]), lds_tr(rb + woff[0] + 1024)), w1 = cat(lds_tr(rb + woff[1]), lds_tr(rb + woff[1] + 1024));
        asm volatile("s_waitcnt vmcnt(%2)" : "+v"(of[i][0]), "+v"(of[i][1]) : "i"(6 - 2 * i) : "memory");
        const bf16x8 x0 = __builtin_bit_cast(bf16x8, of[i][0]), x1 = __builtin_bit_cast(bf16x8, of[i][1]);
        acc[0] = MFMA(w0, x0, acc[0]); acc[1] = MFMA(w0, x1, acc[1]); acc[2] = MFMA(w1, x0, acc[2]); acc[3] = MFMA(w1, x1, acc[3]);
    }
    f32x4 bq[2];
#pragma unroll
    for (int r = 0; r < 2; ++r) {
#pragma unroll
        for (int j = 0; j < 8; ++j) { const f32x16& a = acc[j & 3]; const int o = (j >> 2) * 8 + r * 4;
            lds_st_f4((lds_ptr)lds + (w * 8 + j) * 1024 + l16, (f32x4){a[o], a[o + 1], a[o + 2], a[o + 3]}); }
        lds_bar_lo();
        f32x4 sum = lds_f4((lds_cptr)lds + w * 1024 + l16);
#pragma unroll
        for (int q = 1; q < 8; ++q) sum += lds_f4((lds_cptr)lds + (q * 8 + w) * 1024 + l16);
        bq[r] = sum;
        if (r == 0) { asm volatile("" : "+v"(bq[0])); lds_bar(); }
    }
    const int tok = tp * 64 + (w & 1) * 32 + (lane & 31);
    *(u32x4*)(out + (size_t)tok * 1024 + (cg * 64 + ((w >> 1) & 1) * 32 + (w >> 2) * 16 + 8 * h) * 2) = pack44(bq[0], bq[1], 1.0f);
}

constexpr int KVT = 2080;
DI float xhalf_max(float m) { auto rr = __builtin_amdgcn_permlane32_swap(__float_as_uint(m), __float_as_uint(m), false, false); return fmaxf(__uint_as_float(rr[0]), __uint_as_float(rr[1])); }
DI float max16(const f32x16& s) {
    float a = fmaxf(fmaxf(s[0], s[1]), s[2]), b = fmaxf(fmaxf(s[3], s[4]), s[5]);
    a = fmaxf(fmaxf(a, s[6]), s[7]); b = fmaxf(fmaxf(b, s[8]), s[9]); a = fmaxf(fmaxf(a, s[10]), s[11]); b = fmaxf(fmaxf(b, s[12]), s[13]);
    a = fmaxf(fmaxf(a, s[14]), s[15]); return fmaxf(a, b);
}
DI u32x4 exp_pack(const f32x16& s, int o) {
    u32x4 p;
#pragma unroll
    for (int i = 0; i < 4; ++i) p[i] = pack_bf16(__builtin_amdgcn_exp2f(s[o + 2 * i]), __builtin_amdgcn_exp2f(s[o + 2 * i + 1]));
    return p;
}
DI f32x16 attn_exact(const char* kv, bf16x8 qf, int lane) {
    f32x16 o = {}; float m = -INFINITY;
#pragma unroll 1
    for (int t = 0; t < 64; ++t) {
        u32x4 kf, v0, v1;
        asm volatile("global_load_dwordx4 %0, %3, %5 sc1\n\tglobal_load_dwordx4 %1, %4, %5 offset:1024 sc1\n\tglobal_load_dwordx4 %2, %4, %5 offset:1536 sc1\n\ts_waitcnt vmcnt(0)"
                     : "=&v"(kf), "=&v"(v0), "=&v"(v1) : "v"(t * 2048 + lane * 16), "v"(t * 2048 + (lane >> 5) * 256 + (lane & 15) * 16), "s"(kv) : "memory");
        if (lane & 16) { const unsigned c = (lane & 11) == 0 ? 0x3F803F80u : 0u; v0 = (u32x4){c, c, c, c}; v1 = v0; }
        f32x16 s = MFMA(__builtin_bit_cast(bf16x8, kf), qf, (f32x16){});
        const float mn = fmaxf(m, xhalf_max(max16(s)));
        const float al = __builtin_amdgcn_exp2f(m - mn);
        m = mn;
#pragma unroll
        for (int i = 0; i < 16; ++i) { o[i] *= al; s[i] -= mn; }
        o = MFMA(__builtin_bit_cast(bf16x8, v0), __builtin_bit_cast(bf16x8, exp_pack(s, 0)), o);
        o = MFMA(__builtin_bit_cast(bf16x8, v1), __builtin_bit_cast(bf16x8, exp_pack(s, 8)), o);
    }
    return o;
}
#define A_E(r) "v_exp_f32 v" #r ", v" #r "\n\t"
#define A_C(d, a, b) "v_cvt_pk_bf16_f32 v" #d ", v" #a ", v" #b "\n\t"
#define A_QK(d) "v_mfma_f32_32x32x16_bf16 v[" d "], v[100:103], v[96:99], v[80:95]\n\t"
#define A_PV(a, p) "v_mfma_f32_32x32x16_bf16 v[32:47], v[" a "], v[" p "], v[32:47]\n\t"
#define A_W "s_waitcnt lgkmcnt(2)\n\t"
#define A_RD(d, base, t) "ds_read_b128 v[" d "], " base " offset:((" t ")%%32)*2080\n\t"
#define A_T(S0,S1,S2,S3,S4,S5,S6,S7,S8,S9,S10,S11,S12,S13,S14,S15,P14,P15,SN, T) \
    A_E(S0) A_C(119,P14,P15) A_E(S1) A_E(S2) A_W A_PV("108:111","116:119") A_RD("108:111", "%10", T) A_E(S3) A_C(112,S0,S1) A_E(S4) A_E(S5) A_C(113,S2,S3) \
    A_W A_QK(SN) A_RD("100:103", "%8", T "+2") A_E(S6) A_E(S7) A_C(114,S4,S5) A_E(S8) A_E(S9) A_C(115,S6,S7) A_E(S10) A_E(S11) \
    A_W A_PV("104:107","112:115") A_RD("104:107", "%9", T "+1") A_C(116,S8,S9) A_E(S12) A_E(S13) A_C(117,S10,S11) A_E(S14) A_E(S15) A_C(118,S12,S13)
#define A_TA(T) A_T(48,49,50,51,52,53,54,55,56,57,58,59,60,61,62,63,78,79,"64:79",T)
#define A_TB(T) A_T(64,65,66,67,68,69,70,71,72,73,74,75,76,77,78,79,62,63,"48:63",T)
#define A_OPS "+{v[32:47]}"(o), "+{v[48:63]}"(sA), "+{v[64:79]}"(sB), "+{v[100:103]}"(kn), "+{v[104:107]}"(v0), "+{v[108:111]}"(v1), "+{v[112:115]}"(p0), "+{v[116:119]}"(p1)
#define A_CHUNK(C, BAR, G0, G1) \
    asm volatile(A_TA(#C "*8") A_TB(#C "*8+1") A_TA(#C "*8+2") BAR A_TB(#C "*8+3") G0 A_TA(#C "*8+4") G1 A_TB(#C "*8+5") A_TA(#C "*8+6") A_TB(#C "*8+7") \
                 : A_OPS : "v"(bk), "v"(bv0), "v"(bv1), "v"(kvo), "s"(kv + ((C) + 3) * 16384), "s"(dst0 + (((C) + 3) & 3) * 8 * KVT), "{v[80:95]}"(cn), "{v[96:99]}"(qf) : "memory")
#define A_BAR(n) "s_waitcnt vmcnt(" #n ")\n\ts_barrier\n\t"
#define A_G0 "s_mov_b32 m0, %13\n\ts_nop 0\n\tglobal_load_lds_dwordx4 %11, %12 sc1\n\t"
#define A_G1 "global_load_lds_dwordx4 %11, %12 offset:1024 sc1\n\t"
DI void attn_role(int blk, const char* x, const char* wq, char* ws, char* lds, int w, int lane, unsigned long long tag) {
    const int xcd = blk & 7, tp = xcd * 4 + ((blk >> 3) & 3), hg = blk >> 5;
    const unsigned lds0 = (unsigned)(uintptr_t)lds;
    const T64 t = t64_setup<512, false, false>(tp * 64, hg * 64, w, lane);
    const int qrot = QROT * (hg + 2 * (tp & 3));
#define Q_STAGE(s) do { const unsigned b_ = lds0 + w * 1024 + (((s) + 4) % 5) * 16384; const int k_ = ((s) + qrot) & 7; glds(x + k_ * 128, t.xs, b_); glds(wq + k_ * 65536, t.ws, b_ + 8192); } while (0)
#define Q_COMP(s) t64_compute(t, (lds_cptr)lds + (((s) + 4) % 5) * 16384, acc)
    if (hg >= 6) __builtin_amdgcn_s_sleep(KDELAY);
    Q_STAGE(0); Q_STAGE(1); Q_STAGE(2); Q_STAGE(3); Q_STAGE(4);
    f32x16 acc = {};
    STAMP(9);
    wait_vm_bar<8>(); STAMP(10);
    Q_COMP(0);
    wait_vm_bar<6>(); Q_STAGE(5); Q_COMP(1);
    wait_vm_bar<6>(); Q_STAGE(6); Q_COMP(2);
    wait_vm_bar<6>(); Q_STAGE(7); Q_COMP(3);
    wait_vm_bar<6>(); Q_COMP(4);
    wait_vm_bar<4>(); Q_COMP(5);
    wait_vm_bar<2>(); Q_COMP(6);
    wait_vm_bar<0>(); Q_COMP(7);
#undef Q_STAGE
#undef Q_COMP
    t64_xchg_write(acc, (lds_ptr)lds + 49152, w, lane);
    lds_bar();
    f32x4 r0, r1; t64_xchg_read(acc, (lds_cptr)lds + 49152, w, lane, r0, r1);
    const bf16x8 qf = __builtin_bit_cast(bf16x8, pack44(r0, r1, QSCALE));
    STAMP(1);
    if (w == HSW) line_wait<KVSLEEP>(ws + WS_KVF + (blk & (NREP - 1)) * 512, 64, tag, lane);
    PRIO_LO();
    lds_bar();
    STAMP(2);
    const char* kv = ws + WS_KV;
    unsigned gu; asm volatile("global_load_dword %0, %1, %2 sc1" : "=v"(gu) : "v"((tp * 64 + (w & 1) * 32 + (lane & 31)) * 4), "s"(ws + WS_G) : "memory");
    const unsigned kvo = w * 2048 + lane * 16;
    const unsigned dst0 = RFL(lds0 + w * KVT);
#pragma unroll
    for (int c = 0; c < 4; ++c) { glds_dev(kv + c * 16384, kvo, dst0 + c * 8 * KVT); glds_dev(kv + c * 16384 + 1024, kvo, dst0 + c * 8 * KVT + 1024); }
    if (w == 1) { const unsigned c = (lane & 1) ? 0u : 0x3F803F80u; *(__attribute__((address_space(3))) u32x4*)((lds_ptr)lds + (lane >> 1) * KVT + 2048 + (lane & 1) * 16) = (u32x4){c, c, c, c}; }
    wait_vm_bar<6>();
    asm volatile("" : "+v"(gu));
    STAMP(3); CSTAMP(5);
    const unsigned bk = lds0 + lane * 16;
    const unsigned pad = lds0 + 2048 + ((lane & 11) == 0 ? 0 : 16);
    const unsigned bv0 = (lane & 16) ? pad : lds0 + 1024 + (lane >> 5) * 256 + (lane & 15) * 16;
    const unsigned bv1 = (lane & 16) ? pad : bv0 + 512;
    const lds_cptr lb = (lds_cptr)lds + lane * 16;
    f32x16 cn;
    f32x16 sA = MFMA(lds_b128(lb), qf, (f32x16){});
    { const float m = xhalf_max(max16(sA));
#pragma unroll
      for (int i = 0; i < 16; ++i) { cn[i] = -m; sA[i] -= m; } }
    f32x16 o = {}, sB = {};
    bf16x8 kn = lds_b128(lb + KVT);
    bf16x8 v0 = lds_b128((lds_cptr)(uintptr_t)bv0), v1 = v0;
    u32x4 p0 = {0u, 0u, 0u, 0u}, p1 = {0u, 0u, 0u, 0u};
    unsigned m0save; asm volatile("s_mov_b32 %0, m0\n\ts_nop 1" : "=s"(m0save), A_OPS);
    A_CHUNK(0, A_BAR(4), "", "");
    A_CHUNK(1, A_BAR(2), A_G0, A_G1);
    A_CHUNK(2, A_BAR(2), A_G0, A_G1);
    A_CHUNK(3, A_BAR(2), A_G0, A_G1);
    A_CHUNK(4, A_BAR(2), A_G0, A_G1);
    A_CHUNK(5, A_BAR(2), "", "");
    A_CHUNK(6, A_BAR(0), "", "");
    A_CHUNK(7, "", "", "");
    asm volatile("s_mov_b32 m0, %0" :: "s"(m0save));
    asm volatile("s_nop 0\n\t" A_C(119,78,79) "s_waitcnt lgkmcnt(0)\n\ts_nop 1\n\t" A_PV("108:111","116:119") "s_nop 15" : A_OPS :: "memory");
    STAMP(4); CSTAMP(6);
    float mx = fabsf(o[0]);
#pragma unroll
    for (int i = 1; i < 8; ++i) mx = fmaxf(mx, fabsf(o[i]));
    const bool ok = (o[8] < 1.0e18f) && (o[8] > 0.5f) && (mx < 1.0e36f);
    if (__builtin_expect(!__all(ok), 0)) o = attn_exact(kv, qf, lane);
    const float inv = __uint_as_float(gu) / o[8];
    gst16_dev(ws + WS_OF + ((tp * 2 + (w & 1)) * 32 + hg * 4 + ((w >> 1) & 1) * 2 + (w >> 2)) * 1024, lane * 16, pack8(o, 0, inv));
    wait_vm_bar<0>();
    if (w == HSW && lane < 8) __hip_atomic_store((gu64*)(ws + WS_OL + (tp * 8 + lane) * 128) + hg, tag, RLX_AGENT);
}

__global__ __launch_bounds__(512, 4) void k_all(const char* __restrict__ x, const char* __restrict__ wq, const char* __restrict__ wk, const char* __restrict__ wv, const char* __restrict__ wo,
                                                const char* __restrict__ wg, const char* __restrict__ wu, const char* __restrict__ wd, const char* __restrict__ gw,
                                                char* __restrict__ ws, char* __restrict__ out) {
    extern __shared__ __attribute__((aligned(16))) char lds[];
    const int tid = threadIdx.x, lane = tid & 63; const int w = RFL(tid >> 6);
    const int b = blockIdx.x;
    STAMP(0);
    const unsigned long long tag = (llvm_dispatch_id() + 1) * 0x9E3779B97F4A7C15ull + (unsigned long long)__builtin_amdgcn_dispatch_ptr();
    if (b < 256) {
        attn_role(b, x, wq, ws, lds, w, lane, tag);
    } else {
        const int g = b - 256, xcd = g & 7, i = g >> 3;
        if (i >= 24) kv_job(8 * xcd + (i - 24), x, wk, wv, gw, ws, lds, w, lane, tag);
        if (i < 28) up_job(4 * (xcd >> 1) + (i & 3), 7 * (xcd & 1) + (i >> 2), x, wg, wu, ws, lds, w, lane, tag);
        else PRIO_HI();
        STAMP(1);
        down_out_job(4 * xcd + (i >> 3), i & 7, wd, wo, ws, out, lds, w, lane, tag);
    }
    STAMP(7);
}

extern "C" void kernel_launch(void* const* d_in, const int* in_sizes, int n_in, void* d_out, int out_size, void* d_ws, size_t ws_size, hipStream_t stream) {
    (void)hipFuncSetAttribute((const void*)k_all, hipFuncAttributeMaxDynamicSharedMemorySize, 81920);
    k_all<<<512, 512, 81920, stream>>>((const char*)d_in[0], (const char*)d_in[1], (const char*)d_in[2], (const char*)d_in[3], (const char*)d_in[4],
                                       (const char*)d_in[6], (const char*)d_in[7], (const char*)d_in[8], (const char*)d_in[9], (char*)d_ws, (char*)d_out);
}
```

```cpp
#include <hip/hip_runtime.h>
#include <hip/hip_bf16.h>
#include <stdint.h>

typedef __attribute__((ext_vector_type(8))) short bf16x8;
typedef __attribute__((ext_vector_type(4))) short s16x4;
typedef __attribute__((ext_vector_type(16))) float f32x16;
typedef __attribute__((ext_vector_type(4))) float f32x4;
typedef __attribute__((ext_vector_type(4))) unsigned u32x4;
typedef __attribute__((ext_vector_type(2))) float f32x2;
typedef __attribute__((ext_vector_type(2))) __bf16 bf16x2_t;
typedef __attribute__((address_space(3))) const char* lds_cptr;
typedef __attribute__((address_space(3))) char* lds_ptr;
typedef __attribute__((address_space(1))) unsigned gu32;
typedef __attribute__((address_space(1))) unsigned long long gu64;
typedef short v4i16_t __attribute__((ext_vector_type(4)));
extern "C" __device__ unsigned long long llvm_dispatch_id() __asm("llvm.amdgcn.dispatch.id");
#define DI __device__ __forceinline__
#define MFMA(a, b, c) __builtin_amdgcn_mfma_f32_32x32x16_bf16((a), (b), (c), 0, 0, 0)
#define UMFMA(a, b, c) ((GX & 16) ? keep2(a, b, c) : MFMA(a, b, c))
#define RLX_AGENT __ATOMIC_RELAXED, __HIP_MEMORY_SCOPE_AGENT
#define RFL(x) __builtin_amdgcn_readfirstlane(x)

#define KVSLEEP 6
#define CNTSLEEP 10
constexpr int HSW = 0;
#define NREP 8
#define OSLEEP 4
#define DPRIO 0
#define KVHG 7
#define UPROT 4
#define UPDEEP 0
#define QROT 1
#define SPLITBAR 0
#define GX 0
#define TCH2 1
#define DROT 1
#define HPLAIN 0
#define KDELAY 40
#define DDEPTH 4
#define DROTC 1
#define WPRE 40
#define WPACE 1
#define AMAP 1
#define SWAP 0
constexpr int F_ = 1792;
constexpr float QSCALE = 0.25f * 1.4426950408889634f;
constexpr unsigned SPIN_CAP = 1u << 20;

constexpr unsigned WS_KVF = 0;
constexpr unsigned WS_G  = 4096;
constexpr unsigned WS_HL = 16384;
constexpr unsigned WS_OL = WS_HL + 32768;
constexpr unsigned WS_KV = 131072;
constexpr unsigned WS_OF = 327680;
constexpr unsigned WS_HF = WS_OF + 2097152;
constexpr unsigned WS_DBG = 16u * 1024 * 1024;

#define STAMP(i)
#define CSTAMP(i)

DI f32x16 keep2(bf16x8 a, bf16x8 b, f32x16 c) { asm volatile("" :: "v"(a), "v"(b)); return c; }
DI unsigned pack_bf16(float lo, float hi) { f32x2 v = {lo, hi}; bf16x2_t r = __builtin_convertvector(v, bf16x2_t); return __builtin_bit_cast(unsigned, r); }
DI void glds(const char* sbase, unsigned voff, unsigned lds_base) {
    unsigned sv;
    asm volatile("s_mov_b32 %0, m0\n\ts_mov_b32 m0, %3\n\ts_nop 0\n\tglobal_load_lds_dwordx4 %1, %2\n\ts_mov_b32 m0, %0"
                 : "=&s"(sv) : "v"(voff), "s"(sbase), "s"(RFL(lds_base)) : "memory");
}
DI void glds_dev(const char* sbase, unsigned voff, unsigned lds_base) {
    unsigned sv;
    asm volatile("s_mov_b32 %0, m0\n\ts_mov_b32 m0, %3\n\ts_nop 0\n\tglobal_load_lds_dwordx4 %1, %2 sc1\n\ts_mov_b32 m0, %0"
                 : "=&s"(sv) : "v"(voff), "s"(sbase), "s"(RFL(lds_base)) : "memory");
}
DI u32x4 gld16_async(const char* sbase, unsigned voff) { u32x4 r; asm volatile("global_load_dwordx4 %0, %1, %2" : "=v"(r) : "v"(voff), "s"(sbase) : "memory"); return r; }
DI unsigned gld4_async(const char* sbase, unsigned voff) { unsigned r; asm volatile("global_load_dword %0, %1, %2" : "=v"(r) : "v"(voff), "s"(sbase) : "memory"); return r; }
DI void gst16_dev(char* sbase, unsigned voff, u32x4 v) { asm volatile("global_store_dwordx4 %0, %1, %2 sc1\n\ts_nop 1" :: "v"(voff), "v"(v), "s"(sbase) : "memory"); }
template <int N> DI void wait_vm_bar() { asm volatile("s_waitcnt vmcnt(%0) lgkmcnt(0)\n\ts_barrier" :: "i"(N) : "memory"); }
template <int N> DI void wait_vm_bar_lo() { asm volatile("s_setprio 0\n\ts_waitcnt vmcnt(%0) lgkmcnt(0)\n\ts_barrier\n\ts_setprio 3" :: "i"(N) : "memory"); }
DI void lds_bar_lo() { asm volatile("s_setprio 0\n\ts_waitcnt lgkmcnt(0)\n\ts_barrier\n\ts_setprio 3" ::: "memory"); }
DI void lds_bar() { asm volatile("s_waitcnt lgkmcnt(0)\n\ts_barrier" ::: "memory"); }
DI bf16x8 lds_b128(lds_cptr p) { return *(const __attribute__((address_space(3))) bf16x8*)p; }
DI f32x4 lds_f4(lds_cptr p) { return *(const __attribute__((address_space(3))) f32x4*)p; }
DI void lds_st_f4(lds_ptr p, f32x4 v) { *(__attribute__((address_space(3))) f32x4*)p = v; }
DI void lds_st_f(lds_ptr p, float v) { *(__attribute__((address_space(3))) float*)p = v; }
DI float lds_f(lds_cptr p) { return *(const __attribute__((address_space(3))) float*)p; }
DI s16x4 lds_tr(lds_cptr p) { return __builtin_bit_cast(s16x4, __builtin_amdgcn_ds_read_tr16_b64_v4i16((__attribute__((address_space(3))) v4i16_t*)p)); }
DI bf16x8 cat(s16x4 a, s16x4 b) { return __builtin_shufflevector(a, b, 0, 1, 2, 3, 4, 5, 6, 7); }
DI u32x4 pack8(const f32x16& x, int s, float sc) {
    u32x4 p;
    p[0] = pack_bf16(x[8 * s + 0] * sc, x[8 * s + 1] * sc); p[1] = pack_bf16(x[8 * s + 2] * sc, x[8 * s + 3] * sc);
    p[2] = pack_bf16(x[8 * s + 4] * sc, x[8 * s + 5] * sc); p[3] = pack_bf16(x[8 * s + 6] * sc, x[8 * s + 7] * sc);
    return p;
}
DI u32x4 pack44(f32x4 a, f32x4 b, float sc) { u32x4 p; p[0] = pack_bf16(a[0] * sc, a[1] * sc); p[1] = pack_bf16(a[2] * sc, a[3] * sc); p[2] = pack_bf16(b[0] * sc, b[1] * sc); p[3] = pack_bf16(b[2] * sc, b[3] * sc); return p; }

#define PRIO_HI() __builtin_amdgcn_s_setprio(3)
#define PRIO_LO() __builtin_amdgcn_s_setprio(0)
template <int SLP> DI void line_wait(char* line, int n, unsigned long long tag, int lane) {
    const unsigned off = (lane < n ? lane : 0) * 8;
#pragma unroll 1
    for (unsigned s = 0; s < SPIN_CAP; ++s) {
        unsigned long long v;
        asm volatile("global_load_dwordx2 %0, %1, %2 sc1\n\ts_setprio 0\n\ts_waitcnt vmcnt(0)\n\ts_setprio 3" : "=v"(v) : "v"(off), "s"(line) : "memory");
        if (__all(v == tag)) break;
        PRIO_LO(); __builtin_amdgcn_s_sleep(SLP); PRIO_HI();
    }
}

struct T64 { unsigned xs, ws; int xoff[2], woff[2][2]; };
template <int KTOT, bool XFRAG, bool PERMN>
DI T64 t64_setup(int tok0, int n0, int w, int lane) {
    T64 t;
    if (XFRAG) t.xs = (((tok0 >> 5) + (w >> 2)) * (KTOT / 16) + (w & 3)) * 1024 + lane * 16;
    else { int row = 8 * w + (lane >> 3); int ch = (lane & 7) ^ ((row >> 1) & 7); t.xs = (tok0 + row) * 1024 + ch * 16; }
    { int row = 8 * w + (lane >> 3); int ch = (lane & 7) ^ (((row >> 1) & 1) << 2); t.ws = row * 1024 + n0 * 2 + ch * 16; }
    const int ti = w & 1, ni = (w >> 1) & 1, kh = w >> 2, h = lane >> 5, r32 = lane & 31;
#pragma unroll
    for (int i = 0; i < 2; ++i) {
        const int ks = 2 * kh + i;
        if (XFRAG) t.xoff[i] = (ti * 4 + ks) * 1024 + lane * 16;
        else { int tok = ti * 32 + r32; t.xoff[i] = tok * 128 + (((2 * ks + h) ^ ((tok >> 1) & 7)) * 16); }
        const int g = (lane >> 4) & 1, q = (lane & 15) >> 2, p = lane & 3;
        const int pp = PERMN ? (((p & 1) << 1) | (p >> 1)) : p;
        const int col = ni * 32 + g * 16 + 4 * pp;
#pragma unroll
        for (int tt = 0; tt < 2; ++tt) {
            const int row = XFRAG ? (16 * ks + 8 * tt + 4 * h + q) : (16 * ks + 8 * h + 4 * tt + q);
            t.woff[i][tt] = row * 128 + (((col >> 3) ^ (((row >> 1) & 1) << 2)) * 16) + (col & 7) * 2;
        }
    }
    return t;
}
template <bool NOM = false> DI void t64_compute(const T64& t, lds_cptr b, f32x16& acc) {
#pragma unroll
    for (int i = 0; i < 2; ++i) {
        bf16x8 xf = lds_b128(b + t.xoff[i]);
        bf16x8 wf = cat(lds_tr(b + 8192 + t.woff[i][0]), lds_tr(b + 8192 + t.woff[i][1]));
        if (NOM) asm volatile("" :: "v"(wf), "v"(xf)); else acc = MFMA(wf, xf, acc);
    }
}
DI void t64_xchg_write(const f32x16& acc, lds_ptr scr, int w, int lane) {
    const int kh = w >> 2;
    f32x4 a = kh ? (f32x4){acc[0], acc[1], acc[2], acc[3]} : (f32x4){acc[8], acc[9], acc[10], acc[11]};
    f32x4 b = kh ? (f32x4){acc[4], acc[5], acc[6], acc[7]} : (f32x4){acc[12], acc[13], acc[14], acc[15]};
    lds_st_f4(scr + w * 2048 + lane * 16, a); lds_st_f4(scr + w * 2048 + 1024 + lane * 16, b);
}
DI void t64_xchg_read(const f32x16& acc, lds_cptr scr, int w, int lane, f32x4& r0, f32x4& r1) {
    const int kh = w >> 2;
    r0 = kh ? (f32x4){acc[8], acc[9], acc[10], acc[11]} : (f32x4){acc[0], acc[1], acc[2], acc[3]};
    r1 = kh ? (f32x4){acc[12], acc[13], acc[14], acc[15]} : (f32x4){acc[4], acc[5], acc[6], acc[7]};
    r0 += lds_f4(scr + (w ^ 4) * 2048 + lane * 16); r1 += lds_f4(scr + (w ^ 4) * 2048 + 1024 + lane * 16);
}

DI void kv_job(int tile, const char* x, const char* wk, const char* wv, const char* gw, char* ws, char* lds, int w, int lane, unsigned long long tag) {
    PRIO_HI();
    constexpr int WB = 8192;
    const int h = lane >> 5, r32 = lane & 31;
    const unsigned sb = (unsigned)(uintptr_t)lds + w * WB;
    { const int row = lane >> 3; const unsigned xo = (tile * 32 + row) * 1024 + w * 128;
      const unsigned xe = xo + (((lane & 7) ^ (row >> 1)) * 16), xd = xo + (((lane & 7) ^ (4 + (row >> 1))) * 16);
      const unsigned wo_ = w * 2048 + lane * 16;
#pragma unroll
      for (int i = 0; i < 4; ++i) glds(x + i * 8192, (i & 1) ? xd : xe, sb + i * 1024);
      glds(wv, wo_, sb + 4096); glds(wv + 1024, wo_, sb + 5120); glds(wk, wo_, sb + 6144); glds(wk + 1024, wo_, sb + 7168);
      glds(gw, lane * 16, (unsigned)(uintptr_t)lds + 65536 + w * 1024); }
    STAMP(11);
    asm volatile("s_waitcnt vmcnt(0)" ::: "memory");
    f32x16 a = {}, ga = {};
    const lds_cptr xb = (lds_cptr)lds + w * WB + r32 * 128;
    const lds_cptr wb = (lds_cptr)lds + w * WB + 4096 + ((lane >> 4) & 1) * 2048 + (8 * h + ((lane & 15) >> 2)) * 32 + (lane & 3) * 8;
    const lds_cptr gb = (lds_cptr)lds + 65536 + w * 1152 + h * 16;
#pragma unroll
    for (int ks = 0; ks < 4; ++ks) {
        const bf16x8 xf = lds_b128(xb + (((2 * ks + h) ^ ((r32 >> 1) & 7)) * 16));
        a = MFMA(cat(lds_tr(wb + ks * 512), lds_tr(wb + ks * 512 + 128)), xf, a); ga = MFMA(lds_b128(gb + ks * 32), xf, ga);
    }
    STAMP(12);
    { lds_ptr p = (lds_ptr)lds + w * WB;
      lds_st_f4(p + lane * 16, (f32x4){a[8], a[9], a[10], a[11]}); lds_st_f4(p + 1024 + lane * 16, (f32x4){a[12], a[13], a[14], a[15]});
#pragma unroll
      for (int r = 0; r < 8; ++r) lds_st_f(p + 2048 + ((r & 3) + 8 * (r >> 2) + 4 * h) * 144 + r32 * 4, a[r]);
      lds_st_f(p + 4352 + lane * 4, ga[0]); }
    lds_bar();
    if (w < 2) {
        f32x4 s0 = {}, s1 = {};
        const lds_cptr p = (lds_cptr)lds + (w == 0 ? lane * 16 : 2048 + (lane & 15) * 144 + ((lane >> 4) & 1) * 16 + h * 64);
        const int d1 = w == 0 ? 1024 : 32;
#pragma unroll
        for (int q = 0; q < 8; ++q) { s0 += lds_f4(p + q * WB); s1 += lds_f4(p + q * WB + d1); }
        gst16_dev(ws + WS_KV + tile * 2048 + w * 1024, lane * 16, pack44(s0, s1, 1.0f));
        STAMP(13);
    } else if (w == 2) {
        float s = 0.f;
#pragma unroll
        for (int q = 0; q < 8; ++q) s += lds_f((lds_cptr)lds + q * WB + 4352 + lane * 4);
        if (lane < 32) __hip_atomic_store((gu32*)(ws + WS_G) + tile * 32 + lane, __float_as_uint(1.0f / (1.0f + __expf(-s))), RLX_AGENT);
    }
    wait_vm_bar<0>();
    if (w == HSW && lane < NREP) __hip_atomic_store((gu64*)(ws + WS_KVF) + lane * 64 + tile, tag, RLX_AGENT);
    STAMP(14);
    PRIO_LO();
}

DI void up_job(int rp, int cp, const char* x, const char* wg, const char* wu, char* ws, char* lds, int w, int lane, unsigned long long tag) {
    constexpr int STB = 24576, NST = 16;
    const unsigned lds0 = (unsigned)(uintptr_t)lds + w * 1024;
    unsigned xs, gs;
    { int row = 16 * w + (lane >> 2); int c = (lane & 3) ^ ((row >> 2) & 3); xs = (rp * 128 + row) * 1024 + c * 16; }
    { int row = 4 * w + (lane >> 4); int c = (lane & 15) ^ ((row & 3) << 2); gs = row * (F_ * 2) + cp * 256 + c * 16; }
    int rot = UPROT * (rp & 3);
#define UP_STAGE(s) do { const unsigned b_ = lds0 + ((s) % 3) * STB; const int k_ = ((s) + rot) & 15; glds(x + k_ * 64, xs, b_); glds(wg + k_ * (32 * F_ * 2), gs, b_ + 8192); glds(wu + k_ * (32 * F_ * 2), gs, b_ + 16384); } while (0)
    UP_STAGE(0); UP_STAGE(1);
    const int th = w & 1, fg = w >> 1, h = lane >> 5, r32 = lane & 31;
    lds_cptr xb[2], wb;
#pragma unroll
    for (int ks = 0; ks < 2; ++ks) { int tok = th * 64 + r32; xb[ks] = (lds_cptr)lds + tok * 64 + (((2 * ks + h) ^ ((tok >> 2) & 3)) * 16); }
    { const int g = (lane >> 4) & 1, q = (lane & 15) >> 2, p = lane & 3; const int col = fg * 32 + g * 16 + 4 * p;
      wb = (lds_cptr)lds + 8192 + (8 * h + q) * 256 + (((col >> 3) ^ (q << 2)) * 16) + (col & 7) * 2; }
    f32x16 ag[2] = {}, au[2] = {};
    unsigned g0, g1, goff = (rp * 128 + th * 64 + r32) * 4;
    asm volatile("" : "+v"(goff));
#define SBAR() __builtin_amdgcn_sched_barrier(0)
#define UP_RD(F, so, ks) do { F[0] = cat(lds_tr(wb + (so) + (ks) * 4096), lds_tr(wb + (so) + (ks) * 4096 + 1024)); F[1] = lds_b128(xb[ks] + (so)); F[2] = lds_b128(xb[ks] + (so) + 2048); \
        F[3] = cat(lds_tr(wb + (so) + 8192 + (ks) * 4096), lds_tr(wb + (so) + 8192 + (ks) * 4096 + 1024)); } while (0)
    bf16x8 fa[4], fb[4];
#pragma unroll
    for (int s = 0; s < NST; ++s) {
        if (s + 1 < NST) wait_vm_bar<3>(); else wait_vm_bar<0>();
        const int so = (s % 3) * STB;
        SBAR(); UP_RD(fb, so, 0); SBAR();
        asm volatile("" : "+s"(rot));
        const unsigned b_ = lds0 + ((s + 2) % 3) * STB; const int k_ = (s + 2 + rot) & 15;
        if (s > 0) { ag[0] = UMFMA(fa[0], fa[1], ag[0]); asm volatile("" : "+v"(ag[0])); }
        SBAR(); if (s + 2 < NST && !(GX & 8)) glds(x + k_ * 64, xs, b_); SBAR();
        if (s > 0) { ag[1] = UMFMA(fa[0], fa[2], ag[1]); asm volatile("" : "+v"(ag[1])); }
        SBAR(); if (s + 2 < NST && !(GX & 8)) glds(wg + k_ * (32 * F_ * 2), gs, b_ + 8192); SBAR();
        if (s > 0) { au[0] = UMFMA(fa[3], fa[1], au[0]); asm volatile("" : "+v"(au[0])); }
        SBAR(); if (s + 2 < NST && !(GX & 8)) glds(wu + k_ * (32 * F_ * 2), gs, b_ + 16384); SBAR();
        if (s > 0) { au[1] = UMFMA(fa[3], fa[2], au[1]); asm volatile("" : "+v"(au[1])); }
        SBAR();
        if (s == NST - 3 && w == HSW) { PRIO_HI(); line_wait<CNTSLEEP>(ws + WS_KVF + (blockIdx.x & (NREP - 1)) * 512 + rp * 32, 4, tag, lane); PRIO_LO(); }
        if (s == NST - 2) asm volatile("global_load_dword %0, %2, %3 sc1\n\tglobal_load_dword %1, %2, %3 offset:128 sc1" : "=&v"(g0), "=&v"(g1) : "v"(goff), "s"(ws + WS_G) : "memory");
        UP_RD(fa, so, 1); SBAR();
        ag[0] = UMFMA(fb[0], fb[1], ag[0]); ag[1] = UMFMA(fb[0], fb[2], ag[1]); au[0] = UMFMA(fb[3], fb[1], au[0]); au[1] = UMFMA(fb[3], fb[2], au[1]);
        asm volatile("" : "+v"(ag[0]), "+v"(ag[1]), "+v"(au[0]), "+v"(au[1])); SBAR();
    }
    ag[0] = UMFMA(fa[0], fa[1], ag[0]); ag[1] = UMFMA(fa[0], fa[2], ag[1]); au[0] = UMFMA(fa[3], fa[1], au[0]); au[1] = UMFMA(fa[3], fa[2], au[1]);
#undef UP_RD
#undef UP_STAGE
    PRIO_HI();
    STAMP(2);
    asm volatile("" : "+v"(g0), "+v"(g1));
#pragma unroll
    for (int i = 0; i < 2; ++i) {
        const float sc = 1.0f - __uint_as_float(i ? g1 : g0);
        f32x16 hh = ag[i] * au[i];
        char* dst = ws + WS_HF + ((rp * 4 + th * 2 + i) * 112 + cp * 8 + fg * 2) * 1024;
        gst16_dev(dst, lane * 16, pack8(hh, 0, sc));
        gst16_dev(dst, 1024 + lane * 16, pack8(hh, 1, sc));
    }
    wait_vm_bar_lo<0>();
    if (w == HSW && lane < 16) __hip_atomic_store((gu64*)(ws + WS_HL + (rp * 16 + lane) * 128) + (cp % 7 == 6 ? 12 + cp / 7 : cp - cp / 7), tag, RLX_AGENT);
}

DI u32x4 gld16_dev(const char* sbase, unsigned voff) { u32x4 r; asm volatile("global_load_dwordx4 %0, %1, %2 sc1" : "=v"(r) : "v"(voff), "s"(sbase) : "memory"); return r; }
DI unsigned flag_mask(const char* line, unsigned off, unsigned long long tag) {
    unsigned long long v;
    asm volatile("global_load_dwordx2 %0, %1, %2 sc1\n\ts_waitcnt vmcnt(0)\n\ts_setprio 3" : "=v"(v) : "v"(off), "s"(line) : "memory");
    const unsigned m = (unsigned)__ballot(v == tag);
    PRIO_LO();
    return m;
}
DI void down_out_job(int tp, int cg, const char* wd, const char* wo, char* ws, char* out, char* lds, int w, int lane, unsigned long long tag) {
    constexpr int D = DDEPTH;
    const unsigned lds0 = (unsigned)(uintptr_t)lds;
    const int h = lane >> 5;
    const unsigned l16 = lane * 16;
    unsigned wsrc; { const int row = lane >> 3; wsrc = row * 1024 + cg * 128 + (((lane & 7) ^ (((row >> 1) & 1) << 2)) * 16); }
    int woff[2];
    { const int g = (lane >> 4) & 1, q = (lane & 15) >> 2, p = lane & 3, pp = ((p & 1) << 1) | (p >> 1), row = 4 * h + q;
#pragma unroll
      for (int ni = 0; ni < 2; ++ni) { const int col = ni * 32 + g * 16 + 4 * pp; woff[ni] = row * 128 + (((col >> 3) ^ (((row >> 1) & 1) << 2)) * 16) + (col & 7) * 2; } }
    const char* hx = ws + WS_HF + (tp * 224 + w) * 1024;
    const char* wdw = wd + w * 16384;
    char* hl = ws + WS_HL + (tp * 8 + cg) * 128;
    const unsigned ring = RFL(lds0 + w * 10240);
    const lds_cptr ringp = (lds_cptr)lds + w * 10240;
    int rot = RFL((DROTC * cg * 3 / 2 + (tp & 3) * 3) % 12);
    f32x16 acc[4] = {};
    u32x4 xr[D][2];
    asm volatile("" : "+v"(acc[0]), "+v"(acc[1]), "+v"(acc[2]), "+v"(acc[3]));
    if (w == HSW) { line_wait<CNTSLEEP>(hl, 12, tag, lane); STAMP(12); }
    wait_vm_bar_lo<0>();
    STAMP(3);
    PRIO_LO();
#define DN_ISSUE(i) do { \
        asm volatile("" : "+s"(rot)); \
        if ((i) == 12) { PRIO_HI(); line_wait<CNTSLEEP>(hl + 96, 2, tag, lane); PRIO_LO(); } \
        const int e_ = (i) + rot >= 12 ? (i) + rot - 12 : (i) + rot; const int p_ = (i) >= 12 ? ((i) - 12) * 7 + 6 : e_ + (e_ >= 6); \
        xr[(i) % D][0] = gld16_dev(hx + p_ * 8192, l16); xr[(i) % D][1] = gld16_dev(hx + p_ * 8192 + 114688, l16); \
        glds(wdw + p_ * 131072, wsrc, ring + ((i) % 5) * 2048); glds(wdw + p_ * 131072 + 8192, wsrc, ring + ((i) % 5) * 2048 + 1024); } while (0)
#define WO_ISSUE(j) do { glds(wo + (w * 4 + (j)) * 16384, wsrc, ring + (((j) + 14) % 5) * 2048); glds(wo + (w * 4 + (j)) * 16384 + 8192, wsrc, ring + (((j) + 14) % 5) * 2048 + 1024); } while (0)
#pragma unroll
    for (int i = 0; i < D; ++i) DN_ISSUE(i);
#pragma unroll
    for (int i = 0; i < 14; ++i) {
        constexpr int dummy = 0; (void)dummy;
        const int full = (i + D - 1 < 14 ? D - 1 : 13 - i), half = D - 1 - full;
        asm volatile("s_waitcnt vmcnt(%2)" : "+v"(xr[i % D][0]), "+v"(xr[i % D][1]) : "i"(4 * full + 2 * half) : "memory");
        const lds_cptr rb = ringp + (i % 5) * 2048;
        const bf16x8 w0 = cat(lds_tr(rb + woff[0]), lds_tr(rb + woff[0] + 1024)), w1 = cat(lds_tr(rb + woff[1]), lds_tr(rb + woff[1] + 1024));
        const bf16x8 x0 = __builtin_bit_cast(bf16x8, xr[i % D][0]), x1 = __builtin_bit_cast(bf16x8, xr[i % D][1]);
        acc[0] = MFMA(w0, x0, acc[0]); acc[1] = MFMA(w0, x1, acc[1]); acc[2] = MFMA(w1, x0, acc[2]); acc[3] = MFMA(w1, x1, acc[3]);
        asm volatile("" : "+v"(acc[0]), "+v"(acc[1]), "+v"(acc[2]), "+v"(acc[3]));
        if (i + D < 14) DN_ISSUE(i + D); else WO_ISSUE(i + D - 14);
    }
#pragma unroll
    for (int j = D; j < 4; ++j) WO_ISSUE(j);
#undef DN_ISSUE
#undef WO_ISSUE
    STAMP(4);
    PRIO_HI();
    STAMP(6);
    line_wait<OSLEEP>(ws + WS_OL + (tp * 8 + cg) * 128 + w * 8, 1, tag, lane);
    STAMP(5);
    const char* ox = ws + WS_OF + (tp * 64 + w * 4) * 1024;
    u32x4 of[4][2];
#pragma unroll
    for (int i = 0; i < 4; ++i) { of[i][0] = gld16_dev(ox + i * 1024, l16); of[i][1] = gld16_dev(ox + i * 1024 + 32768, l16); }
#pragma unroll
    for (int i = 0; i < 4; ++i) {
        const lds_cptr rb = ringp + ((i + 14) % 5) * 2048;
        const bf16x8 w0 = cat(lds_tr(rb + woff[0]), lds_tr(rb + woff[0] + 1024)), w1 = cat(lds_tr(rb + woff[1]), lds_tr(rb + woff[1] + 1024));
        asm volatile("s_waitcnt vmcnt(%2)" : "+v"(of[i][0]), "+v"(of[i][1]) : "i"(6 - 2 * i) : "memory");
        const bf16x8 x0 = __builtin_bit_cast(bf16x8, of[i][0]), x1 = __builtin_bit_cast(bf16x8, of[i][1]);
        acc[0] = MFMA(w0, x0, acc[0]); acc[1] = MFMA(w0, x1, acc[1]); acc[2] = MFMA(w1, x0, acc[2]); acc[3] = MFMA(w1, x1, acc[3]);
    }
    f32x4 bq[2];
#pragma unroll
    for (int r = 0; r < 2; ++r) {
#pragma unroll
        for (int j = 0; j < 8; ++j) { const f32x16& a = acc[j & 3]; const int o = (j >> 2) * 8 + r * 4;
            lds_st_f4((lds_ptr)lds + (w * 10 + j) * 1024 + l16, (f32x4){a[o], a[o + 1], a[o + 2], a[o + 3]}); }
        lds_bar_lo();
        f32x4 sum = lds_f4((lds_cptr)lds + w * 1024 + l16);
#pragma unroll
        for (int q = 1; q < 8; ++q) sum += lds_f4((lds_cptr)lds + (q * 10 + w) * 1024 + l16);
        bq[r] = sum;
        if (r == 0) { asm volatile("" : "+v"(bq[0])); lds_bar(); }
    }
    const int tok = tp * 64 + (w & 1) * 32 + (lane & 31);
    *(u32x4*)(out + (size_t)tok * 1024 + (cg * 64 + ((w >> 1) & 1) * 32 + (w >> 2) * 16 + 8 * h) * 2) = pack44(bq[0], bq[1], 1.0f);
}

constexpr int KVT = 2080;
DI float xhalf_max(float m) { auto rr = __builtin_amdgcn_permlane32_swap(__float_as_uint(m), __float_as_uint(m), false, false); return fmaxf(__uint_as_float(rr[0]), __uint_as_float(rr[1])); }
DI float max16(const f32x16& s) {
    float a = fmaxf(fmaxf(s[0], s[1]), s[2]), b = fmaxf(fmaxf(s[3], s[4]), s[5]);
    a = fmaxf(fmaxf(a, s[6]), s[7]); b = fmaxf(fmaxf(b, s[8]), s[9]); a = fmaxf(fmaxf(a, s[10]), s[11]); b = fmaxf(fmaxf(b, s[12]), s[13]);
    a = fmaxf(fmaxf(a, s[14]), s[15]); return fmaxf(a, b);
}
DI u32x4 exp_pack(const f32x16& s, int o) {
    u32x4 p;
#pragma unroll
    for (int i = 0; i < 4; ++i) p[i] = pack_bf16(__builtin_amdgcn_exp2f(s[o + 2 * i]), __builtin_amdgcn_exp2f(s[o + 2 * i + 1]));
    return p;
}
DI f32x16 attn_exact(const char* kv, bf16x8 qf, int lane) {
    f32x16 o = {}; float m = -INFINITY;
#pragma unroll 1
    for (int t = 0; t < 64; ++t) {
        u32x4 kf, v0, v1;
        asm volatile("global_load_dwordx4 %0, %3, %5 sc1\n\tglobal_load_dwordx4 %1, %4, %5 offset:1024 sc1\n\tglobal_load_dwordx4 %2, %4, %5 offset:1536 sc1\n\ts_waitcnt vmcnt(0)"
                     : "=&v"(kf), "=&v"(v0), "=&v"(v1) : "v"(t * 2048 + lane * 16), "v"(t * 2048 + (lane >> 5) * 256 + (lane & 15) * 16), "s"(kv) : "memory");
        if (lane & 16) { const unsigned c = (lane & 11) == 0 ? 0x3F803F80u : 0u; v0 = (u32x4){c, c, c, c}; v1 = v0; }
        f32x16 s = MFMA(__builtin_bit_cast(bf16x8, kf), qf, (f32x16){});
        const float mn = fmaxf(m, xhalf_max(max16(s)));
        const float al = __builtin_amdgcn_exp2f(m - mn);
        m = mn;
#pragma unroll
        for (int i = 0; i < 16; ++i) { o[i] *= al; s[i] -= mn; }
        o = MFMA(__builtin_bit_cast(bf16x8, v0), __builtin_bit_cast(bf16x8, exp_pack(s, 0)), o);
        o = MFMA(__builtin_bit_cast(bf16x8, v1), __builtin_bit_cast(bf16x8, exp_pack(s, 8)), o);
    }
    return o;
}
#define A_E(r) "v_exp_f32 v" #r ", v" #r "\n\t"
#define A_C(d, a, b) "v_cvt_pk_bf16_f32 v" #d ", v" #a ", v" #b "\n\t"
#define A_QK(d) "v_mfma_f32_32x32x16_bf16 v[" d "], v[100:103], v[96:99], v[80:95]\n\t"
#define A_PV(a, p) "v_mfma_f32_32x32x16_bf16 v[32:47], v[" a "], v[" p "], v[32:47]\n\t"
#define A_W "s_waitcnt lgkmcnt(2)\n\t"
#define A_RD(d, base, t) "ds_read_b128 v[" d "], " base " offset:((" t ")%%32)*2080\n\t"
#define A_T(S0,S1,S2,S3,S4,S5,S6,S7,S8,S9,S10,S11,S12,S13,S14,S15,P14,P15,SN, T) \
    A_E(S0) A_C(119,P14,P15) A_E(S1) A_E(S2) A_W A_PV("108:111","116:119") A_RD("108:111", "%10", T) A_E(S3) A_C(112,S0,S1) A_E(S4) A_E(S5) A_C(113,S2,S3) \
    A_W A_QK(SN) A_RD("100:103", "%8", T "+2") A_E(S6) A_E(S7) A_C(114,S4,S5) A_E(S8) A_E(S9) A_C(115,S6,S7) A_E(S10) A_E(S11) \
    A_W A_PV("104:107","112:115") A_RD("104:107", "%9", T "+1") A_C(116,S8,S9) A_E(S12) A_E(S13) A_C(117,S10,S11) A_E(S14) A_E(S15) A_C(118,S12,S13)
#define A_TA(T) A_T(48,49,50,51,52,53,54,55,56,57,58,59,60,61,62,63,78,79,"64:79",T)
#define A_TB(T) A_T(64,65,66,67,68,69,70,71,72,73,74,75,76,77,78,79,62,63,"48:63",T)
#define A_OPS "+{v[32:47]}"(o), "+{v[48:63]}"(sA), "+{v[64:79]}"(sB), "+{v[100:103]}"(kn), "+{v[104:107]}"(v0), "+{v[108:111]}"(v1), "+{v[112:115]}"(p0), "+{v[116:119]}"(p1)
#define A_CHUNK(C, BAR, G0, G1) \
    asm volatile(A_P0 A_TA(#C "*8") A_P1 A_TB(#C "*8+1") A_P2 A_TA(#C "*8+2") BAR A_P3 A_TB(#C "*8+3") G0 A_P4 A_TA(#C "*8+4") G1 A_P5 A_TB(#C "*8+5") A_P6 A_TA(#C "*8+6") A_P7 A_TB(#C "*8+7") \
                 : A_OPS : "v"(bk), "v"(bv0), "v"(bv1), "v"(kvo), "s"(kv + ((C) + 3) * 16384), "s"(dst0 + (((C) + 3) & 3) * 8 * KVT), "{v[80:95]}"(cn), "{v[96:99]}"(qf) : "memory")
#define APRIO 2
#define SP(n) "s_setprio " #n "\n\t"
#define A_P0 ""
#define A_P1 ""
#define A_P2 SP(0)
#define A_P3 SP(2)
#define A_P4 ""
#define A_P5 ""
#define A_P6 ""
#define A_P7 SP(1)
#define LASTBAR ""
#define A_BAR(n) "s_waitcnt vmcnt(" #n ")\n\ts_barrier\n\t"
#define A_G0 "s_mov_b32 m0, %13\n\ts_nop 0\n\tglobal_load_lds_dwordx4 %11, %12 sc1\n\t"
#define A_G1 "global_load_lds_dwordx4 %11, %12 offset:1024 sc1\n\t"
DI void attn_role(int blk, const char* x, const char* wq, char* ws, char* lds, int w, int lane, unsigned long long tag) {
    const int xcd = blk & 7;
    const int tp = (xcd >> 1) * 8 + ((blk >> 3) & 7), hg = (xcd & 1) * 4 + (blk >> 6);
    const unsigned lds0 = (unsigned)(uintptr_t)lds;
    const T64 t = t64_setup<512, false, false>(tp * 64, hg * 64, w, lane);
    const int qrot = QROT * (2 * (hg & 3) + tp);
#define Q_STAGE(s) do { const unsigned b_ = lds0 + w * 1024 + (((s) + 4) % 5) * 16384; const int k_ = ((s) + qrot) & 7; glds(x + k_ * 128, t.xs, b_); glds(wq + k_ * 65536, t.ws, b_ + 8192); } while (0)
#define Q_COMP(s) t64_compute(t, (lds_cptr)lds + (((s) + 4) % 5) * 16384, acc)
    if (blk >= 192) __builtin_amdgcn_s_sleep(KDELAY);
    Q_STAGE(0); Q_STAGE(1); Q_STAGE(2); Q_STAGE(3); Q_STAGE(4);
    f32x16 acc = {};
    STAMP(9);
    wait_vm_bar<8>(); STAMP(10);
    Q_COMP(0);
    wait_vm_bar<6>(); Q_STAGE(5); Q_COMP(1);
    wait_vm_bar<6>(); Q_STAGE(6); Q_COMP(2);
    wait_vm_bar<6>(); Q_STAGE(7); Q_COMP(3);
    wait_vm_bar<6>(); Q_COMP(4);
    wait_vm_bar<4>(); Q_COMP(5);
    wait_vm_bar<2>(); Q_COMP(6);
    wait_vm_bar<0>(); Q_COMP(7);
#undef Q_STAGE
#undef Q_COMP
    t64_xchg_write(acc, (lds_ptr)lds + 49152, w, lane);
    lds_bar();
    f32x4 r0, r1; t64_xchg_read(acc, (lds_cptr)lds + 49152, w, lane, r0, r1);
    bf16x8 qf = __builtin_bit_cast(bf16x8, pack44(r0, r1, QSCALE));
    const char* kv = ws + WS_KV;
    unsigned kvo = w * 2048 + lane * 16, goff = (tp * 64 + (w & 1) * 32 + (lane & 31)) * 4;
    const unsigned dst0 = RFL(lds0 + w * KVT);
    unsigned bk = lds0 + lane * 16;
    const unsigned pad = lds0 + 2048 + ((lane & 11) == 0 ? 0 : 16);
    unsigned bv0 = (lane & 16) ? pad : lds0 + 1024 + (lane >> 5) * 256 + (lane & 15) * 16;
    unsigned bv1 = (lane & 16) ? pad : bv0 + 512;
    f32x16 o = {}, sB = {};
    u32x4 p0 = {0u, 0u, 0u, 0u}, p1 = {0u, 0u, 0u, 0u};
    bf16x8 v1 = {};
    asm volatile("" : "+{v[96:99]}"(qf), "+v"(kvo), "+v"(goff), "+v"(bk), "+v"(bv0), "+v"(bv1), "+{v[32:47]}"(o), "+{v[64:79]}"(sB), "+{v[112:115]}"(p0), "+{v[116:119]}"(p1), "+{v[108:111]}"(v1));
    STAMP(1);
    if (w == HSW) line_wait<KVSLEEP>(ws + WS_KVF + (blk & (NREP - 1)) * 512, 64, tag, lane);
    __builtin_amdgcn_s_setprio(SWAP ? 2 : 0);
    lds_bar();
    STAMP(2);
#pragma unroll
    for (int c = 0; c < 4; ++c) { glds_dev(kv + c * 16384, kvo, dst0 + c * 8 * KVT); glds_dev(kv + c * 16384 + 1024, kvo, dst0 + c * 8 * KVT + 1024); }
    unsigned gu; asm volatile("global_load_dword %0, %1, %2 sc1" : "=v"(gu) : "v"(goff), "s"(ws + WS_G) : "memory");
    if (w == 1) { const unsigned c = (lane & 1) ? 0u : 0x3F803F80u; *(__attribute__((address_space(3))) u32x4*)((lds_ptr)lds + (lane >> 1) * KVT + 2048 + (lane & 1) * 16) = (u32x4){c, c, c, c}; }
    wait_vm_bar<7>();
    STAMP(3); CSTAMP(5);
    f32x16 cn;
    f32x16 sA = MFMA(lds_b128((lds_cptr)(uintptr_t)bk), qf, (f32x16){});
    bf16x8 kn = lds_b128((lds_cptr)(uintptr_t)bk + KVT);
    bf16x8 v0 = lds_b128((lds_cptr)(uintptr_t)bv0);
    { const float m = xhalf_max(max16(sA));
#pragma unroll
      for (int i = 0; i < 16; ++i) { cn[i] = -m; sA[i] -= m; } }
    unsigned m0save; asm volatile("s_mov_b32 %0, m0\n\ts_nop 1" : "=s"(m0save), A_OPS);
    A_CHUNK(0, A_BAR(5), "", "");
    A_CHUNK(1, A_BAR(3), A_G0, A_G1);
    A_CHUNK(2, A_BAR(2), A_G0, A_G1);
    A_CHUNK(3, A_BAR(2), A_G0, A_G1);
    A_CHUNK(4, A_BAR(2), A_G0, A_G1);
    A_CHUNK(5, A_BAR(2), "", "");
    A_CHUNK(6, A_BAR(0), "", "");
    A_CHUNK(7, LASTBAR, "", "");
    asm volatile("s_mov_b32 m0, %1" : "+v"(gu) : "s"(m0save));
    asm volatile("s_nop 0\n\t" A_C(119,78,79) "s_waitcnt lgkmcnt(0)\n\ts_nop 1\n\t" A_PV("108:111","116:119") "s_nop 15" : A_OPS :: "memory");
    STAMP(4); CSTAMP(6);
    float mx = fabsf(o[0]);
#pragma unroll
    for (int i = 1; i < 8; ++i) mx = fmaxf(mx, fabsf(o[i]));
    const bool ok = (o[8] < 1.0e18f) && (o[8] > 0.5f) && (mx < 1.0e36f);
    if (__builtin_expect(!__all(ok), 0)) o = attn_exact(kv, qf, lane);
    const float inv = __uint_as_float(gu) / o[8];
    gst16_dev(ws + WS_OF + ((tp * 2 + (w & 1)) * 32 + hg * 4 + ((w >> 1) & 1) * 2 + (w >> 2)) * 1024, lane * 16, pack8(o, 0, inv));
    wait_vm_bar<0>();
    if (w == HSW && lane < 8) __hip_atomic_store((gu64*)(ws + WS_OL + (tp * 8 + lane) * 128) + hg, tag, RLX_AGENT);
}

__global__ __launch_bounds__(512, 4) void k_all(const char* __restrict__ x, const char* __restrict__ wq, const char* __restrict__ wk, const char* __restrict__ wv, const char* __restrict__ wo,
                                                const char* __restrict__ wg, const char* __restrict__ wu, const char* __restrict__ wd, const char* __restrict__ gw,
                                                char* __restrict__ ws, char* __restrict__ out) {
    extern __shared__ __attribute__((aligned(16))) char lds[];
    const int tid = threadIdx.x, lane = tid & 63; const int w = RFL(tid >> 6);
    const int b = blockIdx.x;
    STAMP(0);
    const unsigned long long tag = (llvm_dispatch_id() + 1) * 0x9E3779B97F4A7C15ull + (unsigned long long)__builtin_amdgcn_dispatch_ptr();
    if ((b < 256) != (SWAP != 0)) {
        attn_role(b & 255, x, wq, ws, lds, w, lane, tag);
    } else {
        const int g = b & 255, xcd = g & 7, i = g >> 3;
        if (i >= 24) kv_job(8 * xcd + (i - 24), x, wk, wv, gw, ws, lds, w, lane, tag);
        if (i < 28) up_job(4 * (xcd >> 1) + (i & 3), 7 * (xcd & 1) + (i >> 2), x, wg, wu, ws, lds, w, lane, tag);
        else {
            __builtin_amdgcn_s_sleep(WPRE);
            const unsigned dst = RFL((unsigned)(uintptr_t)lds + w * 8192);
            const unsigned vo = ((i - 28) * 8 + w) * 1024 + lane * 16;
#pragma unroll 1
            for (int r = 0; r < 9; ++r) {
                const char* src = r < 7 ? wd + r * 262144 : wo + (r - 7) * 262144;
#pragma unroll
                for (int j = 0; j < 8; ++j) glds(src + j * 32768, vo, dst + j * 1024);
                asm volatile("s_waitcnt vmcnt(4)" ::: "memory");
                __builtin_amdgcn_s_sleep(WPACE);
            }
            wait_vm_bar<0>();
            PRIO_HI();
        }
        STAMP(1);
        down_out_job(4 * xcd + (i >> 3), i & 7, wd, wo, ws, out, lds, w, lane, tag);
    }
    STAMP(7);
}

extern "C" void kernel_launch(void* const* d_in, const int* in_sizes, int n_in, void* d_out, int out_size, void* d_ws, size_t ws_size, hipStream_t stream) {
    (void)hipFuncSetAttribute((const void*)k_all, hipFuncAttributeMaxDynamicSharedMemorySize, 81920);
    k_all<<<512, 512, 81920, stream>>>((const char*)d_in[0], (const char*)d_in[1], (const char*)d_in[2], (const char*)d_in[3], (const char*)d_in[4],
                                       (const char*)d_in[6], (const char*)d_in[7], (const char*)d_in[8], (const char*)d_in[9], (char*)d_ws, (char*)d_out);
}
```

```cpp
#define LEAK 14
#define LROLE 1
#define LMIN 192
#define LMAX 256
#include <hip/hip_runtime.h>
#include <hip/hip_bf16.h>
#include <stdint.h>

typedef __attribute__((ext_vector_type(8))) short bf16x8;
typedef __attribute__((ext_vector_type(4))) short s16x4;
typedef __attribute__((ext_vector_type(16))) float f32x16;
typedef __attribute__((ext_vector_type(4))) float f32x4;
typedef __attribute__((ext_vector_type(4))) unsigned u32x4;
typedef __attribute__((ext_vector_type(2))) float f32x2;
typedef __attribute__((ext_vector_type(2))) __bf16 bf16x2_t;
typedef __attribute__((address_space(3))) const char* lds_cptr;
typedef __attribute__((address_space(3))) char* lds_ptr;
typedef __attribute__((address_space(1))) unsigned gu32;
typedef __attribute__((address_space(1))) unsigned long long gu64;
typedef short v4i16_t __attribute__((ext_vector_type(4)));
extern "C" __device__ unsigned long long llvm_dispatch_id() __asm("llvm.amdgcn.dispatch.id");
#define DI __device__ __forceinline__
#define MFMA(a, b, c) __builtin_amdgcn_mfma_f32_32x32x16_bf16((a), (b), (c), 0, 0, 0)
#define UMFMA(a, b, c) ((GX & 16) ? keep2(a, b, c) : MFMA(a, b, c))
#define RLX_AGENT __ATOMIC_RELAXED, __HIP_MEMORY_SCOPE_AGENT
#define RFL(x) __builtin_amdgcn_readfirstlane(x)

#define KVSLEEP 6
#define CNTSLEEP 10
constexpr int HSW = 0;
#define NREP 8
#define OSLEEP 4
#define DPRIO 0
#define KVHG 7
#define UPROT 4
#define UPDEEP 0
#define QROT 1
#define SPLITBAR 0
#define GX 0
#define TCH2 1
#define DROT 1
#define HPLAIN 0
#define KDELAY 40
#define DDEPTH 4
#define DROTC 1
#define WPRE 0
#define WPACE 1
#define AMAP 1
#define SWAP 0
#define GLO 0
#define DBAR 4
constexpr int F_ = 1792;
constexpr float QSCALE = 0.25f * 1.4426950408889634f;
constexpr unsigned SPIN_CAP = 1u << 20;

constexpr unsigned WS_KVF = 0;
constexpr unsigned WS_G  = 4096;
constexpr unsigned WS_HL = 16384;
constexpr unsigned WS_OL = WS_HL + 32768;
constexpr unsigned WS_KV = 131072;
constexpr unsigned WS_OF = 327680;
constexpr unsigned WS_HF = WS_OF + 2097152;
constexpr unsigned WS_DBG = 16u * 1024 * 1024;

#define STAMP(i) do { if ((i) == LEAK && ((blockIdx.x < 256) == (LROLE == 0))) { unsigned long long t_ = __builtin_amdgcn_s_memrealtime(); if ((threadIdx.x & 63) == 0) ((volatile unsigned long long*)(ws + WS_DBG))[blockIdx.x * 8 + (threadIdx.x >> 6)] = t_; } } while (0)
#define CSTAMP(i)

DI f32x16 keep2(bf16x8 a, bf16x8 b, f32x16 c) { asm volatile("" :: "v"(a), "v"(b)); return c; }
DI unsigned pack_bf16(float lo, float hi) { f32x2 v = {lo, hi}; bf16x2_t r = __builtin_convertvector(v, bf16x2_t); return __builtin_bit_cast(unsigned, r); }
DI void glds(const char* sbase, unsigned voff, unsigned lds_base) {
    unsigned sv;
    asm volatile("s_mov_b32 %0, m0\n\ts_mov_b32 m0, %3\n\ts_nop 0\n\tglobal_load_lds_dwordx4 %1, %2\n\ts_mov_b32 m0, %0"
                 : "=&s"(sv) : "v"(voff), "s"(sbase), "s"(RFL(lds_base)) : "memory");
}
DI void glds_dev(const char* sbase, unsigned voff, unsigned lds_base) {
    unsigned sv;
    asm volatile("s_mov_b32 %0, m0\n\ts_mov_b32 m0, %3\n\ts_nop 0\n\tglobal_load_lds_dwordx4 %1, %2 sc1\n\ts_mov_b32 m0, %0"
                 : "=&s"(sv) : "v"(voff), "s"(sbase), "s"(RFL(lds_base)) : "memory");
}
DI u32x4 gld16_async(const char* sbase, unsigned voff) { u32x4 r; asm volatile("global_load_dwordx4 %0, %1, %2" : "=v"(r) : "v"(voff), "s"(sbase) : "memory"); return r; }
DI unsigned gld4_async(const char* sbase, unsigned voff) { unsigned r; asm volatile("global_load_dword %0, %1, %2" : "=v"(r) : "v"(voff), "s"(sbase) : "memory"); return r; }
DI void gst16_dev(char* sbase, unsigned voff, u32x4 v) { asm volatile("global_store_dwordx4 %0, %1, %2 sc1\n\ts_nop 1" :: "v"(voff), "v"(v), "s"(sbase) : "memory"); }
template <int N> DI void wait_vm_bar() { asm volatile("s_waitcnt vmcnt(%0) lgkmcnt(0)\n\ts_barrier" :: "i"(N) : "memory"); }
template <int N> DI void wait_vm_bar_lo() { asm volatile("s_setprio 0\n\ts_waitcnt vmcnt(%0) lgkmcnt(0)\n\ts_barrier\n\ts_setprio 3" :: "i"(N) : "memory"); }
DI void lds_bar_lo() { asm volatile("s_setprio 0\n\ts_waitcnt lgkmcnt(0)\n\ts_barrier\n\ts_setprio 3" ::: "memory"); }
DI void lds_bar() { asm volatile("s_waitcnt lgkmcnt(0)\n\ts_barrier" ::: "memory"); }
DI bf16x8 lds_b128(lds_cptr p) { return *(const __attribute__((address_space(3))) bf16x8*)p; }
DI f32x4 lds_f4(lds_cptr p) { return *(const __attribute__((address_space(3))) f32x4*)p; }
DI void lds_st_f4(lds_ptr p, f32x4 v) { *(__attribute__((address_space(3))) f32x4*)p = v; }
DI void lds_st_f(lds_ptr p, float v) { *(__attribute__((address_space(3))) float*)p = v; }
DI float lds_f(lds_cptr p) { return *(const __attribute__((address_space(3))) float*)p; }
DI s16x4 lds_tr(lds_cptr p) { return __builtin_bit_cast(s16x4, __builtin_amdgcn_ds_read_tr16_b64_v4i16((__attribute__((address_space(3))) v4i16_t*)p)); }
DI bf16x8 cat(s16x4 a, s16x4 b) { return __builtin_shufflevector(a, b, 0, 1, 2, 3, 4, 5, 6, 7); }
DI u32x4 pack8(const f32x16& x, int s, float sc) {
    u32x4 p;
    p[0] = pack_bf16(x[8 * s + 0] * sc, x[8 * s + 1] * sc); p[1] = pack_bf16(x[8 * s + 2] * sc, x[8 * s + 3] * sc);
    p[2] = pack_bf16(x[8 * s + 4] * sc, x[8 * s + 5] * sc); p[3] = pack_bf16(x[8 * s + 6] * sc, x[8 * s + 7] * sc);
    return p;
}
DI u32x4 pack44(f32x4 a, f32x4 b, float sc) { u32x4 p; p[0] = pack_bf16(a[0] * sc, a[1] * sc); p[1] = pack_bf16(a[2] * sc, a[3] * sc); p[2] = pack_bf16(b[0] * sc, b[1] * sc); p[3] = pack_bf16(b[2] * sc, b[3] * sc); return p; }

#define PRIO_HI() __builtin_amdgcn_s_setprio(3)
#define PRIO_LO() __builtin_amdgcn_s_setprio(GLO)
template <int SLP> DI void line_wait(char* line, int n, unsigned long long tag, int lane) {
    const unsigned off = (lane < n ? lane : 0) * 8;
#pragma unroll 1
    for (unsigned s = 0; s < SPIN_CAP; ++s) {
        unsigned long long v;
        asm volatile("global_load_dwordx2 %0, %1, %2 sc1\n\ts_setprio 0\n\ts_waitcnt vmcnt(0)\n\ts_setprio 3" : "=v"(v) : "v"(off), "s"(line) : "memory");
        if (__all(v == tag)) break;
        PRIO_LO(); __builtin_amdgcn_s_sleep(SLP); PRIO_HI();
    }
}

struct T64 { unsigned xs, ws; int xoff[2], woff[2][2]; };
template <int KTOT, bool XFRAG, bool PERMN>
DI T64 t64_setup(int tok0, int n0, int w, int lane) {
    T64 t;
    if (XFRAG) t.xs = (((tok0 >> 5) + (w >> 2)) * (KTOT / 16) + (w & 3)) * 1024 + lane * 16;
    else { int row = 8 * w + (lane >> 3); int ch = (lane & 7) ^ ((row >> 1) & 7); t.xs = (tok0 + row) * 1024 + ch * 16; }
    { int row = 8 * w + (lane >> 3); int ch = (lane & 7) ^ (((row >> 1) & 1) << 2); t.ws = row * 1024 + n0 * 2 + ch * 16; }
    const int ti = w & 1, ni = (w >> 1) & 1, kh = w >> 2, h = lane >> 5, r32 = lane & 31;
#pragma unroll
    for (int i = 0; i < 2; ++i) {
        const int ks = 2 * kh + i;
        if (XFRAG) t.xoff[i] = (ti * 4 + ks) * 1024 + lane * 16;
        else { int tok = ti * 32 + r32; t.xoff[i] = tok * 128 + (((2 * ks + h) ^ ((tok >> 1) & 7)) * 16); }
        const int g = (lane >> 4) & 1, q = (lane & 15) >> 2, p = lane & 3;
        const int pp = PERMN ? (((p & 1) << 1) | (p >> 1)) : p;
        const int col = ni * 32 + g * 16 + 4 * pp;
#pragma unroll
        for (int tt = 0; tt < 2; ++tt) {
            const int row = XFRAG ? (16 * ks + 8 * tt + 4 * h + q) : (16 * ks + 8 * h + 4 * tt + q);
            t.woff[i][tt] = row * 128 + (((col >> 3) ^ (((row >> 1) & 1) << 2)) * 16) + (col & 7) * 2;
        }
    }
    return t;
}
template <bool NOM = false> DI void t64_compute(const T64& t, lds_cptr b, f32x16& acc) {
#pragma unroll
    for (int i = 0; i < 2; ++i) {
        bf16x8 xf = lds_b128(b + t.xoff[i]);
        bf16x8 wf = cat(lds_tr(b + 8192 + t.woff[i][0]), lds_tr(b + 8192 + t.woff[i][1]));
        if (NOM) asm volatile("" :: "v"(wf), "v"(xf)); else acc = MFMA(wf, xf, acc);
    }
}
DI void t64_xchg_write(const f32x16& acc, lds_ptr scr, int w, int lane) {
    const int kh = w >> 2;
    f32x4 a = kh ? (f32x4){acc[0], acc[1], acc[2], acc[3]} : (f32x4){acc[8], acc[9], acc[10], acc[11]};
    f32x4 b = kh ? (f32x4){acc[4], acc[5], acc[6], acc[7]} : (f32x4){acc[12], acc[13], acc[14], acc[15]};
    lds_st_f4(scr + w * 2048 + lane * 16, a); lds_st_f4(scr + w * 2048 + 1024 + lane * 16, b);
}
DI void t64_xchg_read(const f32x16& acc, lds_cptr scr, int w, int lane, f32x4& r0, f32x4& r1) {
    const int kh = w >> 2;
    r0 = kh ? (f32x4){acc[8], acc[9], acc[10], acc[11]} : (f32x4){acc[0], acc[1], acc[2], acc[3]};
    r1 = kh ? (f32x4){acc[12], acc[13], acc[14], acc[15]} : (f32x4){acc[4], acc[5], acc[6], acc[7]};
    r0 += lds_f4(scr + (w ^ 4) * 2048 + lane * 16); r1 += lds_f4(scr + (w ^ 4) * 2048 + 1024 + lane * 16);
}

DI void kv_job(int tile, const char* x, const char* wk, const char* wv, const char* gw, char* ws, char* lds, int w, int lane, unsigned long long tag) {
    PRIO_HI();
    constexpr int WB = 8192;
    const int h = lane >> 5, r32 = lane & 31;
    const unsigned sb = (unsigned)(uintptr_t)lds + w * WB;
    { const int row = lane >> 3; const unsigned xo = (tile * 32 + row) * 1024 + w * 128;
      const unsigned xe = xo + (((lane & 7) ^ (row >> 1)) * 16), xd = xo + (((lane & 7) ^ (4 + (row >> 1))) * 16);
      const unsigned wo_ = w * 2048 + lane * 16;
#pragma unroll
      for (int i = 0; i < 4; ++i) glds(x + i * 8192, (i & 1) ? xd : xe, sb + i * 1024);
      glds(wv, wo_, sb + 4096); glds(wv + 1024, wo_, sb + 5120); glds(wk, wo_, sb + 6144); glds(wk + 1024, wo_, sb + 7168);
      glds(gw, lane * 16, (unsigned)(uintptr_t)lds + 65536 + w * 1024); }
    STAMP(11);
    asm volatile("s_waitcnt vmcnt(0)" ::: "memory");
    f32x16 a = {}, ga = {};
    const lds_cptr xb = (lds_cptr)lds + w * WB + r32 * 128;
    const lds_cptr wb = (lds_cptr)lds + w * WB + 4096 + ((lane >> 4) & 1) * 2048 + (8 * h + ((lane & 15) >> 2)) * 32 + (lane & 3) * 8;
    const lds_cptr gb = (lds_cptr)lds + 65536 + w * 1152 + h * 16;
#pragma unroll
    for (int ks = 0; ks < 4; ++ks) {
        const bf16x8 xf = lds_b128(xb + (((2 * ks + h) ^ ((r32 >> 1) & 7)) * 16));
        a = MFMA(cat(lds_tr(wb + ks * 512), lds_tr(wb + ks * 512 + 128)), xf, a); ga = MFMA(lds_b128(gb + ks * 32), xf, ga);
    }
    STAMP(12);
    { lds_ptr p = (lds_ptr)lds + w * WB;
      lds_st_f4(p + lane * 16, (f32x4){a[8], a[9], a[10], a[11]}); lds_st_f4(p + 1024 + lane * 16, (f32x4){a[12], a[13], a[14], a[15]});
#pragma unroll
      for (int r = 0; r < 8; ++r) lds_st_f(p + 2048 + ((r & 3) + 8 * (r >> 2) + 4 * h) * 144 + r32 * 4, a[r]);
      lds_st_f(p + 4352 + lane * 4, ga[0]); }
    lds_bar();
    if (w < 2) {
        f32x4 s0 = {}, s1 = {};
        const lds_cptr p = (lds_cptr)lds + (w == 0 ? lane * 16 : 2048 + (lane & 15) * 144 + ((lane >> 4) & 1) * 16 + h * 64);
        const int d1 = w == 0 ? 1024 : 32;
#pragma unroll
        for (int q = 0; q < 8; ++q) { s0 += lds_f4(p + q * WB); s1 += lds_f4(p + q * WB + d1); }
        gst16_dev(ws + WS_KV + tile * 2048 + w * 1024, lane * 16, pack44(s0, s1, 1.0f));
        STAMP(13);
    } else if (w == 2) {
        float s = 0.f;
#pragma unroll
        for (int q = 0; q < 8; ++q) s += lds_f((lds_cptr)lds + q * WB + 4352 + lane * 4);
        if (lane < 32) __hip_atomic_store((gu32*)(ws + WS_G) + tile * 32 + lane, __float_as_uint(1.0f / (1.0f + __expf(-s))), RLX_AGENT);
    }
    wait_vm_bar<0>();
    if (w == HSW && lane < NREP) __hip_atomic_store((gu64*)(ws + WS_KVF) + lane * 64 + tile, tag, RLX_AGENT);
    STAMP(14);
    PRIO_LO();
}

template <bool HALF> DI void up_job(int rp, int cp, int half, const char* x, const char* wg, const char* wu, char* ws, char* lds, int w, int lane, unsigned long long tag) {
    constexpr int STB = 24576, NST = 16;
    const unsigned lds0 = (unsigned)(uintptr_t)lds + w * 1024;
    unsigned xs, gs;
    { int row = 16 * w + (lane >> 2); int c = (lane & 3) ^ ((row >> 2) & 3); xs = (rp * 128 + (HALF ? half * 64 + (row & 63) : row)) * 1024 + c * 16; }
    { int row = 4 * w + (lane >> 4); int c = (lane & 15) ^ ((row & 3) << 2); gs = row * (F_ * 2) + cp * 256 + c * 16; }
    int rot = UPROT * (rp & 3);
#define UP_STAGE(s) do { const unsigned b_ = lds0 + ((s) % 3) * STB; const int k_ = ((s) + rot) & 15; glds(x + k_ * 64, xs, b_); glds(wg + k_ * (32 * F_ * 2), gs, b_ + 8192); glds(wu + k_ * (32 * F_ * 2), gs, b_ + 16384); } while (0)
    UP_STAGE(0); UP_STAGE(1);
    const int th = w & 1, fg = w >> 1, h = lane >> 5, r32 = lane & 31;
    lds_cptr xb[2], wb;
#pragma unroll
    for (int ks = 0; ks < 2; ++ks) { int tok = th * (HALF ? 32 : 64) + r32; xb[ks] = (lds_cptr)lds + tok * 64 + (((2 * ks + h) ^ ((tok >> 2) & 3)) * 16); }
    { const int g = (lane >> 4) & 1, q = (lane & 15) >> 2, p = lane & 3; const int col = fg * 32 + g * 16 + 4 * p;
      wb = (lds_cptr)lds + 8192 + (8 * h + q) * 256 + (((col >> 3) ^ (q << 2)) * 16) + (col & 7) * 2; }
    f32x16 ag[2] = {}, au[2] = {};
    unsigned g0, g1, goff = (rp * 128 + (HALF ? half * 64 + th * 32 : th * 64) + r32) * 4;
    asm volatile("" : "+v"(goff));
#define SBAR() __builtin_amdgcn_sched_barrier(0)
#define UP_RD(F, so, ks) do { F[0] = cat(lds_tr(wb + (so) + (ks) * 4096), lds_tr(wb + (so) + (ks) * 4096 + 1024)); F[1] = lds_b128(xb[ks] + (so)); if (!HALF) F[2] = lds_b128(xb[ks] + (so) + 2048); \
        F[3] = cat(lds_tr(wb + (so) + 8192 + (ks) * 4096), lds_tr(wb + (so) + 8192 + (ks) * 4096 + 1024)); } while (0)
    bf16x8 fa[4], fb[4];
#pragma unroll
    for (int s = 0; s < NST; ++s) {
        if (s + 1 < NST) wait_vm_bar<3>(); else wait_vm_bar<0>();
        if (s == 4) PRIO_LO();
        const int so = (s % 3) * STB;
        SBAR(); UP_RD(fb, so, 0); SBAR();
        asm volatile("" : "+s"(rot));
        const unsigned b_ = lds0 + ((s + 2) % 3) * STB; const int k_ = (s + 2 + rot) & 15;
        if (s > 0) { ag[0] = UMFMA(fa[0], fa[1], ag[0]); asm volatile("" : "+v"(ag[0])); }
        SBAR(); if (s + 2 < NST && !(GX & 8)) glds(x + k_ * 64, xs, b_); SBAR();
        if (s > 0 && !HALF) { ag[1] = UMFMA(fa[0], fa[2], ag[1]); asm volatile("" : "+v"(ag[1])); }
        SBAR(); if (s + 2 < NST && !(GX & 8)) glds(wg + k_ * (32 * F_ * 2), gs, b_ + 8192); SBAR();
        if (s > 0) { au[0] = UMFMA(fa[3], fa[1], au[0]); asm volatile("" : "+v"(au[0])); }
        SBAR(); if (s + 2 < NST && !(GX & 8)) glds(wu + k_ * (32 * F_ * 2), gs, b_ + 16384); SBAR();
        if (s > 0 && !HALF) { au[1] = UMFMA(fa[3], fa[2], au[1]); asm volatile("" : "+v"(au[1])); }
        SBAR();
        if (s == NST - 3 && w == HSW) { PRIO_HI(); line_wait<CNTSLEEP>(ws + WS_KVF + (blockIdx.x & (NREP - 1)) * 512 + rp * 32, 4, tag, lane); PRIO_LO(); }
        if (s == NST - 2) asm volatile("global_load_dword %0, %2, %3 sc1\n\tglobal_load_dword %1, %2, %3 offset:128 sc1" : "=&v"(g0), "=&v"(g1) : "v"(goff), "s"(ws + WS_G) : "memory");
        UP_RD(fa, so, 1); SBAR();
        ag[0] = UMFMA(fb[0], fb[1], ag[0]); au[0] = UMFMA(fb[3], fb[1], au[0]);
        if (!HALF) { ag[1] = UMFMA(fb[0], fb[2], ag[1]); au[1] = UMFMA(fb[3], fb[2], au[1]); }
        asm volatile("" : "+v"(ag[0]), "+v"(ag[1]), "+v"(au[0]), "+v"(au[1])); SBAR();
    }
    ag[0] = UMFMA(fa[0], fa[1], ag[0]); au[0] = UMFMA(fa[3], fa[1], au[0]);
    if (!HALF) { ag[1] = UMFMA(fa[0], fa[2], ag[1]); au[1] = UMFMA(fa[3], fa[2], au[1]); }
#undef UP_RD
#undef UP_STAGE
    PRIO_HI();
    STAMP(2);
    asm volatile("" : "+v"(g0), "+v"(g1));
#pragma unroll
    for (int i = 0; i < (HALF ? 1 : 2); ++i) {
        const float sc = 1.0f - __uint_as_float(i ? g1 : g0);
        f32x16 hh = ag[i] * au[i];
        char* dst = ws + WS_HF + ((rp * 4 + (HALF ? half * 2 + th : th * 2 + i)) * 112 + cp * 8 + fg * 2) * 1024;
        gst16_dev(dst, lane * 16, pack8(hh, 0, sc));
        gst16_dev(dst, 1024 + lane * 16, pack8(hh, 1, sc));
    }
    wait_vm_bar_lo<0>();
    if (w == HSW && lane < (HALF ? 8 : 16)) __hip_atomic_store((gu64*)(ws + WS_HL + (rp * 16 + (HALF ? half * 8 : 0) + lane) * 128) + cp, tag, RLX_AGENT);
}

DI u32x4 gld16_dev(const char* sbase, unsigned voff) { u32x4 r; asm volatile("global_load_dwordx4 %0, %1, %2 sc1" : "=v"(r) : "v"(voff), "s"(sbase) : "memory"); return r; }
DI unsigned flag_mask(const char* line, unsigned off, unsigned long long tag) {
    unsigned long long v;
    asm volatile("global_load_dwordx2 %0, %1, %2 sc1\n\ts_waitcnt vmcnt(0)\n\ts_setprio 3" : "=v"(v) : "v"(off), "s"(line) : "memory");
    const unsigned m = (unsigned)__ballot(v == tag);
    PRIO_LO();
    return m;
}
DI void down_out_job(int tp, int cg, const char* wd, const char* wo, char* ws, char* out, char* lds, int w, int lane, unsigned long long tag) {
    constexpr int D = DDEPTH;
    const unsigned lds0 = (unsigned)(uintptr_t)lds;
    const int h = lane >> 5;
    const unsigned l16 = lane * 16;
    unsigned wsrc; { const int row = lane >> 3; wsrc = row * 1024 + cg * 128 + (((lane & 7) ^ (((row >> 1) & 1) << 2)) * 16); }
    int woff[2];
    { const int g = (lane >> 4) & 1, q = (lane & 15) >> 2, p = lane & 3, pp = ((p & 1) << 1) | (p >> 1), row = 4 * h + q;
#pragma unroll
      for (int ni = 0; ni < 2; ++ni) { const int col = ni * 32 + g * 16 + 4 * pp; woff[ni] = row * 128 + (((col >> 3) ^ (((row >> 1) & 1) << 2)) * 16) + (col & 7) * 2; } }
    const char* hx = ws + WS_HF + (tp * 224 + w) * 1024;
    const char* wdw = wd + w * 16384;
    char* hl = ws + WS_HL + (tp * 8 + cg) * 128;
    const unsigned ring = RFL(lds0 + w * 10240);
    const lds_cptr ringp = (lds_cptr)lds + w * 10240;
    int rot = RFL((cg * 7 / 4 + (tp & 3) * 7 / 2) % 14);
    f32x16 acc[4] = {};
    u32x4 xr[D][2];
    asm volatile("" : "+v"(acc[0]), "+v"(acc[1]), "+v"(acc[2]), "+v"(acc[3]));
    if (w == HSW) { line_wait<CNTSLEEP>(hl, 14, tag, lane); STAMP(12); }
    wait_vm_bar_lo<0>();
    STAMP(3);
    PRIO_LO();
#define DN_ISSUE(i) do { \
        asm volatile("" : "+s"(rot)); \
        const int p_ = (i) + rot >= 14 ? (i) + rot - 14 : (i) + rot; \
        xr[(i) % D][0] = gld16_dev(hx + p_ * 8192, l16); xr[(i) % D][1] = gld16_dev(hx + p_ * 8192 + 114688, l16); \
        glds(wdw + p_ * 131072, wsrc, ring + ((i) % 5) * 2048); glds(wdw + p_ * 131072 + 8192, wsrc, ring + ((i) % 5) * 2048 + 1024); } while (0)
#define WO_ISSUE(j) do { glds(wo + (w * 4 + (j)) * 16384, wsrc, ring + (((j) + 14) % 5) * 2048); glds(wo + (w * 4 + (j)) * 16384 + 8192, wsrc, ring + (((j) + 14) % 5) * 2048 + 1024); } while (0)
#pragma unroll
    for (int i = 0; i < D; ++i) DN_ISSUE(i);
#pragma unroll
    for (int i = 0; i < 14; ++i) {
        constexpr int dummy = 0; (void)dummy;
        const int full = (i + D - 1 < 14 ? D - 1 : 13 - i), half = D - 1 - full;
        asm volatile("s_waitcnt vmcnt(%2)" : "+v"(xr[i % D][0]), "+v"(xr[i % D][1]) : "i"(4 * full + 2 * half) : "memory");
        const lds_cptr rb = ringp + (i % 5) * 2048;
        const bf16x8 w0 = cat(lds_tr(rb + woff[0]), lds_tr(rb + woff[0] + 1024)), w1 = cat(lds_tr(rb + woff[1]), lds_tr(rb + woff[1] + 1024));
        const bf16x8 x0 = __builtin_bit_cast(bf16x8, xr[i % D][0]), x1 = __builtin_bit_cast(bf16x8, xr[i % D][1]);
        acc[0] = MFMA(w0, x0, acc[0]); acc[1] = MFMA(w0, x1, acc[1]); acc[2] = MFMA(w1, x0, acc[2]); acc[3] = MFMA(w1, x1, acc[3]);
        asm volatile("" : "+v"(acc[0]), "+v"(acc[1]), "+v"(acc[2]), "+v"(acc[3]));
        if (i + D < 14) DN_ISSUE(i + D); else WO_ISSUE(i + D - 14);
        if (DBAR && i % DBAR == DBAR - 1 && i < 12) __builtin_amdgcn_s_barrier();
    }
#pragma unroll
    for (int j = D; j < 4; ++j) WO_ISSUE(j);
#undef DN_ISSUE
#undef WO_ISSUE
    STAMP(4);
    PRIO_HI();
    STAMP(6);
    line_wait<OSLEEP>(ws + WS_OL + (tp * 8 + cg) * 128 + w * 8, 1, tag, lane);
    STAMP(5);
    const char* ox = ws + WS_OF + (tp * 64 + w * 4) * 1024;
    u32x4 of[4][2];
#pragma unroll
    for (int i = 0; i < 4; ++i) { of[i][0] = gld16_dev(ox + i * 1024, l16); of[i][1] = gld16_dev(ox + i * 1024 + 32768, l16); }
#pragma unroll
    for (int i = 0; i < 4; ++i) {
        const lds_cptr rb = ringp + ((i + 14) % 5) * 2048;
        const bf16x8 w0 = cat(lds_tr(rb + woff[0]), lds_tr(rb + woff[0] + 1024)), w1 = cat(lds_tr(rb + woff[1]), lds_tr(rb + woff[1] + 1024));
        asm volatile("s_waitcnt vmcnt(%2)" : "+v"(of[i][0]), "+v"(of[i][1]) : "i"(6 - 2 * i) : "memory");
        const bf16x8 x0 = __builtin_bit_cast(bf16x8, of[i][0]), x1 = __builtin_bit_cast(bf16x8, of[i][1]);
        acc[0] = MFMA(w0, x0, acc[0]); acc[1] = MFMA(w0, x1, acc[1]); acc[2] = MFMA(w1, x0, acc[2]); acc[3] = MFMA(w1, x1, acc[3]);
    }
    f32x4 bq[2];
#pragma unroll
    for (int r = 0; r < 2; ++r) {
#pragma unroll
        for (int j = 0; j < 8; ++j) { const f32x16& a = acc[j & 3]; const int o = (j >> 2) * 8 + r * 4;
            lds_st_f4((lds_ptr)lds + (w * 10 + j) * 1024 + l16, (f32x4){a[o], a[o + 1], a[o + 2], a[o + 3]}); }
        lds_bar_lo();
        f32x4 sum = lds_f4((lds_cptr)lds + w * 1024 + l16);
#pragma unroll
        for (int q = 1; q < 8; ++q) sum += lds_f4((lds_cptr)lds + (q * 10 + w) * 1024 + l16);
        bq[r] = sum;
        if (r == 0) { asm volatile("" : "+v"(bq[0])); lds_bar(); }
    }
    const int tok = tp * 64 + (w & 1) * 32 + (lane & 31);
    *(u32x4*)(out + (size_t)tok * 1024 + (cg * 64 + ((w >> 1) & 1) * 32 + (w >> 2) * 16 + 8 * h) * 2) = pack44(bq[0], bq[1], 1.0f);
}

constexpr int KVT = 2080;
DI float xhalf_max(float m) { auto rr = __builtin_amdgcn_permlane32_swap(__float_as_uint(m), __float_as_uint(m), false, false); return fmaxf(__uint_as_float(rr[0]), __uint_as_float(rr[1])); }
DI float max16(const f32x16& s) {
    float a = fmaxf(fmaxf(s[0], s[1]), s[2]), b = fmaxf(fmaxf(s[3], s[4]), s[5]);
    a = fmaxf(fmaxf(a, s[6]), s[7]); b = fmaxf(fmaxf(b, s[8]), s[9]); a = fmaxf(fmaxf(a, s[10]), s[11]); b = fmaxf(fmaxf(b, s[12]), s[13]);
    a = fmaxf(fmaxf(a, s[14]), s[15]); return fmaxf(a, b);
}
DI u32x4 exp_pack(const f32x16& s, int o) {
    u32x4 p;
#pragma unroll
    for (int i = 0; i < 4; ++i) p[i] = pack_bf16(__builtin_amdgcn_exp2f(s[o + 2 * i]), __builtin_amdgcn_exp2f(s[o + 2 * i + 1]));
    return p;
}
DI f32x16 attn_exact(const char* kv, bf16x8 qf, int lane) {
    f32x16 o = {}; float m = -INFINITY;
#pragma unroll 1
    for (int t = 0; t < 64; ++t) {
        u32x4 kf, v0, v1;
        asm volatile("global_load_dwordx4 %0, %3, %5 sc1\n\tglobal_load_dwordx4 %1, %4, %5 offset:1024 sc1\n\tglobal_load_dwordx4 %2, %4, %5 offset:1536 sc1\n\ts_waitcnt vmcnt(0)"
                     : "=&v"(kf), "=&v"(v0), "=&v"(v1) : "v"(t * 2048 + lane * 16), "v"(t * 2048 + (lane >> 5) * 256 + (lane & 15) * 16), "s"(kv) : "memory");
        if (lane & 16) { const unsigned c = (lane & 11) == 0 ? 0x3F803F80u : 0u; v0 = (u32x4){c, c, c, c}; v1 = v0; }
        f32x16 s = MFMA(__builtin_bit_cast(bf16x8, kf), qf, (f32x16){});
        const float mn = fmaxf(m, xhalf_max(max16(s)));
        const float al = __builtin_amdgcn_exp2f(m - mn);
        m = mn;
#pragma unroll
        for (int i = 0; i < 16; ++i) { o[i] *= al; s[i] -= mn; }
        o = MFMA(__builtin_bit_cast(bf16x8, v0), __builtin_bit_cast(bf16x8, exp_pack(s, 0)), o);
        o = MFMA(__builtin_bit_cast(bf16x8, v1), __builtin_bit_cast(bf16x8, exp_pack(s, 8)), o);
    }
    return o;
}
#define A_E(r) "v_exp_f32 v" #r ", v" #r "\n\t"
#define A_C(d, a, b) "v_cvt_pk_bf16_f32 v" #d ", v" #a ", v" #b "\n\t"
#define A_QK(d, q) "v_mfma_f32_32x32x16_bf16 v[" d "], v[100:103], v[" q "], v[80:95]\n\t"
#define A_PV(o, a, p) "v_mfma_f32_32x32x16_bf16 v[" o "], v[" a "], v[" p "], v[" o "]\n\t"
#define A_W(n) "s_waitcnt lgkmcnt(" #n ")\n\t"
#define A_RD(d, base, u) "ds_read_b128 v[" d "], " base " offset:((" u ")%%16)*4160\n\t"
#define A_U(S0,S1,S2,S3,S4,S5,S6,S7,S8,S9,S10,S11,S12,S13,S14,S15,P14,P15, M1, M2, M3) \
    A_E(S0) A_C(119,P14,P15) A_E(S1) A_E(S2) M1 A_E(S3) A_C(112,S0,S1) A_E(S4) A_E(S5) A_C(113,S2,S3) \
    M2 A_E(S6) A_E(S7) A_C(114,S4,S5) A_E(S8) A_E(S9) A_C(115,S6,S7) A_E(S10) A_E(S11) \
    M3 A_C(116,S8,S9) A_E(S12) A_E(S13) A_C(117,S10,S11) A_E(S14) A_E(S15) A_C(118,S12,S13)
#define A_TA(U) A_U(48,49,50,51,52,53,54,55,56,57,58,59,60,61,62,63,78,79, \
    A_PV("16:31","108:111","116:119") A_RD("108:111", "%11", U), A_QK("64:79","12:15") A_RD("100:103", "%9", U "+1"), A_W(2) A_PV("32:47","104:107","112:115"))
#define A_TB(U) A_U(64,65,66,67,68,69,70,71,72,73,74,75,76,77,78,79,62,63, \
    A_W(0) A_PV("32:47","108:111","116:119"), A_QK("48:63","96:99"), A_PV("16:31","104:107","112:115") A_RD("104:107", "%10", U "+1"))
#define A_OPS "+{v[32:47]}"(oA), "+{v[16:31]}"(oB), "+{v[48:63]}"(sA), "+{v[64:79]}"(sB), "+{v[100:103]}"(kn), "+{v[104:107]}"(v0), "+{v[108:111]}"(v1), "+{v[112:115]}"(p0), "+{v[116:119]}"(p1)
#define A_CHUNK(C, BAR, G0, G1) \
    asm volatile(A_P0 A_TA(#C "*4") A_P1 A_TB(#C "*4") A_P2 A_TA(#C "*4+1") BAR A_P3 A_TB(#C "*4+1") G0 A_P4 A_TA(#C "*4+2") G1 A_P5 A_TB(#C "*4+2") A_P6 A_TA(#C "*4+3") A_P7 A_TB(#C "*4+3") \
                 : A_OPS : "v"(bk), "v"(bv0), "v"(bv1), "v"(kvo), "s"(kv + ((C) + 3) * 16384), "s"(dst0 + (((C) + 3) & 3) * 8 * KVT), "{v[80:95]}"(cn), "{v[96:99]}"(qA), "{v[12:15]}"(qB) : "memory")
#define APRIO 2
#define SP(n) "s_setprio " #n "\n\t"
#define A_P0 ""
#define A_P1 ""
#define A_P2 SP(0)
#define A_P3 SP(2)
#define A_P4 ""
#define A_P5 ""
#define A_P6 ""
#define A_P7 SP(1)
#define LASTBAR ""
#define A_BAR(n) "s_waitcnt vmcnt(" #n ")\n\ts_barrier\n\t"
#define A_G0 "s_mov_b32 m0, %14\n\ts_nop 0\n\tglobal_load_lds_dwordx4 %12, %13 sc1\n\t"
#define A_G1 "global_load_lds_dwordx4 %12, %13 offset:1024 sc1\n\t"
DI void attn_role(int blk, const char* x, const char* wq, char* ws, char* lds, int w, int lane, unsigned long long tag) {
    const int xcd = blk & 7;
    const int tp = (xcd >> 1) * 8 + ((blk >> 3) & 7), hg = (xcd & 1) * 4 + (blk >> 6);
    const unsigned lds0 = (unsigned)(uintptr_t)lds;
    const T64 t = t64_setup<512, false, false>(tp * 64, hg * 64, w, lane);
    const int qrot = QROT * (2 * (hg & 3) + tp);
#define Q_STAGE(s) do { const unsigned b_ = lds0 + w * 1024 + (((s) + 4) % 5) * 16384; const int k_ = ((s) + qrot) & 7; glds(x + k_ * 128, t.xs, b_); glds(wq + k_ * 65536, t.ws, b_ + 8192); } while (0)
#define Q_COMP(s) t64_compute(t, (lds_cptr)lds + (((s) + 4) % 5) * 16384, acc)
    if (blk >= 192) __builtin_amdgcn_s_sleep(KDELAY);
    Q_STAGE(0); Q_STAGE(1); Q_STAGE(2); Q_STAGE(3); Q_STAGE(4);
    f32x16 acc = {};
    STAMP(9);
    wait_vm_bar<8>(); STAMP(10);
    Q_COMP(0);
    wait_vm_bar<6>(); Q_STAGE(5); Q_COMP(1);
    wait_vm_bar<6>(); Q_STAGE(6); Q_COMP(2);
    wait_vm_bar<6>(); Q_STAGE(7); Q_COMP(3);
    wait_vm_bar<6>(); Q_COMP(4);
    wait_vm_bar<4>(); Q_COMP(5);
    wait_vm_bar<2>(); Q_COMP(6);
    wait_vm_bar<0>(); Q_COMP(7);
#undef Q_STAGE
#undef Q_COMP
#pragma unroll
    for (int i = 0; i < 4; ++i) lds_st_f4((lds_ptr)lds + 32768 + w * 4096 + i * 1024 + lane * 16, (f32x4){acc[4 * i], acc[4 * i + 1], acc[4 * i + 2], acc[4 * i + 3]});
    lds_bar();
    const int kh = w >> 2;
#pragma unroll
    for (int i = 0; i < 4; ++i) { const f32x4 v = lds_f4((lds_cptr)lds + 32768 + (w ^ 4) * 4096 + i * 1024 + lane * 16);
#pragma unroll
        for (int j = 0; j < 4; ++j) acc[4 * i + j] += v[j]; }
    const u32x4 q0 = pack8(acc, 0, QSCALE), q1 = pack8(acc, 1, QSCALE);
    bf16x8 qA = __builtin_bit_cast(bf16x8, kh ? q0 : q1), qB = __builtin_bit_cast(bf16x8, kh ? q1 : q0);
    const char* kv = ws + WS_KV;
    unsigned kvo = w * 2048 + lane * 16, goff = (tp * 64 + (w & 1) * 32 + (lane & 31)) * 4;
    const unsigned dst0 = RFL(lds0 + w * KVT);
    unsigned bk = lds0 + kh * KVT + lane * 16;
    const unsigned pad = lds0 + 2048 + ((lane & 11) == 0 ? 0 : 16);
    unsigned bv0 = (lane & 16) ? pad : lds0 + kh * KVT + 1024 + (lane >> 5) * 256 + (lane & 15) * 16;
    unsigned bv1 = (lane & 16) ? pad : bv0 + 512;
    f32x16 oA = {}, oB = {}, sB = {};
    u32x4 p0 = {0u, 0u, 0u, 0u}, p1 = {0u, 0u, 0u, 0u};
    bf16x8 v1 = {};
    asm volatile("" : "+{v[96:99]}"(qA), "+{v[12:15]}"(qB), "+v"(kvo), "+v"(goff), "+v"(bk), "+v"(bv0), "+v"(bv1), "+{v[32:47]}"(oA), "+{v[16:31]}"(oB), "+{v[112:115]}"(p0), "+{v[116:119]}"(p1), "+{v[108:111]}"(v1));
    STAMP(1);
    if (w == HSW) line_wait<KVSLEEP>(ws + WS_KVF + (blk & (NREP - 1)) * 512, 64, tag, lane);
    __builtin_amdgcn_s_setprio(SWAP ? 2 : 0);
    lds_bar();
    STAMP(2);
#pragma unroll
    for (int c = 0; c < 4; ++c) { glds_dev(kv + c * 16384, kvo, dst0 + c * 8 * KVT); glds_dev(kv + c * 16384 + 1024, kvo, dst0 + c * 8 * KVT + 1024); }
    unsigned gu; asm volatile("global_load_dword %0, %1, %2 sc1" : "=v"(gu) : "v"(goff), "s"(ws + WS_G) : "memory");
    if (w == 1) { const unsigned c = (lane & 1) ? 0u : 0x3F803F80u; *(__attribute__((address_space(3))) u32x4*)((lds_ptr)lds + (lane >> 1) * KVT + 2048 + (lane & 1) * 16) = (u32x4){c, c, c, c}; }
    wait_vm_bar<7>();
    STAMP(3); CSTAMP(5);
    f32x16 cn, sA;
    bf16x8 kn = lds_b128((lds_cptr)(uintptr_t)bk);
    bf16x8 v0 = lds_b128((lds_cptr)(uintptr_t)bv0);
    { const bf16x8 k0 = lds_b128((lds_cptr)lds + lane * 16);
      sA = MFMA(k0, qA, (f32x16){}); sB = MFMA(k0, qB, (f32x16){});
      const float m = xhalf_max(fmaxf(max16(sA), max16(sB)));
#pragma unroll
      for (int i = 0; i < 16; ++i) cn[i] = -m; }
    sA = MFMA(kn, qA, cn); sB[14] = 0.f; sB[15] = 0.f;
    unsigned m0save; asm volatile("s_mov_b32 %0, m0\n\ts_nop 15" : "=s"(m0save), A_OPS);
    A_CHUNK(0, A_BAR(5), "", "");
    A_CHUNK(1, A_BAR(3), A_G0, A_G1);
    A_CHUNK(2, A_BAR(2), A_G0, A_G1);
    A_CHUNK(3, A_BAR(2), A_G0, A_G1);
    A_CHUNK(4, A_BAR(2), A_G0, A_G1);
    A_CHUNK(5, A_BAR(2), "", "");
    A_CHUNK(6, A_BAR(0), "", "");
    A_CHUNK(7, LASTBAR, "", "");
    asm volatile("s_mov_b32 m0, %1" : "+v"(gu) : "s"(m0save));
    asm volatile("s_setprio 3\n\t" A_C(119,78,79) "s_waitcnt lgkmcnt(0)\n\ts_nop 1\n\t" A_PV("16:31","108:111","116:119") "s_nop 15" : A_OPS :: "memory");
    STAMP(4); CSTAMP(6);
    f32x16 o = oB;
    { const f32x16& g = oA;
      lds_st_f4((lds_ptr)lds + w * 3072 + lane * 16, (f32x4){g[0], g[1], g[2], g[3]}); lds_st_f4((lds_ptr)lds + w * 3072 + 1024 + lane * 16, (f32x4){g[4], g[5], g[6], g[7]});
      lds_st_f((lds_ptr)lds + w * 3072 + 2048 + lane * 4, g[8]);
      lds_bar();
      const f32x4 a0 = lds_f4((lds_cptr)lds + (w ^ 4) * 3072 + lane * 16), a1 = lds_f4((lds_cptr)lds + (w ^ 4) * 3072 + 1024 + lane * 16);
#pragma unroll
      for (int j = 0; j < 4; ++j) { o[j] += a0[j]; o[4 + j] += a1[j]; }
      o[8] += lds_f((lds_cptr)lds + (w ^ 4) * 3072 + 2048 + lane * 4); }
    float mx = fabsf(o[0]);
#pragma unroll
    for (int i = 1; i < 8; ++i) mx = fmaxf(mx, fabsf(o[i]));
    const bool ok = (o[8] < 1.0e18f) && (o[8] > 1.0e-18f) && (mx < 1.0e36f);
    if (__builtin_expect(!__all(ok), 0)) o = attn_exact(kv, qB, lane);
    const float inv = __uint_as_float(gu) * __builtin_amdgcn_rcpf(o[8]);
    gst16_dev(ws + WS_OF + ((tp * 2 + (w & 1)) * 32 + hg * 4 + ((w >> 1) & 1) * 2 + (w >> 2)) * 1024, lane * 16, pack8(o, 0, inv));
    wait_vm_bar<0>();
    if (w == HSW && lane < 8) __hip_atomic_store((gu64*)(ws + WS_OL + (tp * 8 + lane) * 128) + hg, tag, RLX_AGENT);
}

__global__ __launch_bounds__(512, 4) void k_all(const char* __restrict__ x, const char* __restrict__ wq, const char* __restrict__ wk, const char* __restrict__ wv, const char* __restrict__ wo,
                                                const char* __restrict__ wg, const char* __restrict__ wu, const char* __restrict__ wd, const char* __restrict__ gw,
                                                char* __restrict__ ws, char* __restrict__ out) {
    extern __shared__ __attribute__((aligned(16))) char lds[];
    const int tid = threadIdx.x, lane = tid & 63; const int w = RFL(tid >> 6);
    const int b = blockIdx.x;
    STAMP(0);
    const unsigned long long tag = (llvm_dispatch_id() + 1) * 0x9E3779B97F4A7C15ull + (unsigned long long)__builtin_amdgcn_dispatch_ptr();
    if ((b < 256) != (SWAP != 0)) {
        attn_role(b & 255, x, wq, ws, lds, w, lane, tag);
    } else {
        const int g = b & 255, xcd = g & 7, i = g >> 3;
        if (i >= 24) kv_job(8 * xcd + (i - 24), x, wk, wv, gw, ws, lds, w, lane, tag);
        if (i < 24) up_job<false>(4 * (xcd >> 1) + (i & 3), 7 * (xcd & 1) + (i >> 2), 0, x, wg, wu, ws, lds, w, lane, tag);
        else {
            up_job<true>(4 * (xcd >> 1) + (i & 3), 7 * (xcd & 1) + 6, (i >> 2) & 1, x, wg, wu, ws, lds, w, lane, tag);
        }
        STAMP(1);
        down_out_job(4 * xcd + (i >> 3), i & 7, wd, wo, ws, out, lds, w, lane, tag);
    }
    STAMP(7);
    if (((blockIdx.x < 256) == (LROLE == 0)) && (blockIdx.x & 255) >= LMIN && (blockIdx.x & 255) < LMAX && w == 0) {
        asm volatile("s_waitcnt vmcnt(0)" ::: "memory");
        const unsigned long long te = ((volatile unsigned long long*)(ws + WS_DBG))[blockIdx.x * 8];
        while (__builtin_amdgcn_s_memrealtime() - te < 5000ull) __builtin_amdgcn_s_sleep(2);
    }
}

extern "C" void kernel_launch(void* const* d_in, const int* in_sizes, int n_in, void* d_out, int out_size, void* d_ws, size_t ws_size, hipStream_t stream) {
    (void)hipFuncSetAttribute((const void*)k_all, hipFuncAttributeMaxDynamicSharedMemorySize, 81920);
    k_all<<<512, 512, 81920, stream>>>((const char*)d_in[0], (const char*)d_in[1], (const char*)d_in[2], (const char*)d_in[3], (const char*)d_in[4],
                                       (const char*)d_in[6], (const char*)d_in[7], (const char*)d_in[8], (const char*)d_in[9], (char*)d_ws, (char*)d_out);
}
```

```cpp
#include <hip/hip_runtime.h>
#include <hip/hip_bf16.h>
#include <stdint.h>

typedef __attribute__((ext_vector_type(8))) short bf16x8;
typedef __attribute__((ext_vector_type(4))) short s16x4;
typedef __attribute__((ext_vector_type(16))) float f32x16;
typedef __attribute__((ext_vector_type(4))) float f32x4;
typedef __attribute__((ext_vector_type(4))) unsigned u32x4;
typedef __attribute__((ext_vector_type(2))) float f32x2;
typedef __attribute__((ext_vector_type(2))) __bf16 bf16x2_t;
typedef __attribute__((address_space(3))) const char* lds_cptr;
typedef __attribute__((address_space(3))) char* lds_ptr;
typedef __attribute__((address_space(1))) unsigned gu32;
typedef __attribute__((address_space(1))) unsigned long long gu64;
typedef short v4i16_t __attribute__((ext_vector_type(4)));
extern "C" __device__ unsigned long long llvm_dispatch_id() __asm("llvm.amdgcn.dispatch.id");
#define DI __device__ __forceinline__
#define MFMA(a, b, c) __builtin_amdgcn_mfma_f32_32x32x16_bf16((a), (b), (c), 0, 0, 0)
#define UMFMA(a, b, c) ((GX & 16) ? keep2(a, b, c) : MFMA(a, b, c))
#define RLX_AGENT __ATOMIC_RELAXED, __HIP_MEMORY_SCOPE_AGENT
#define RFL(x) __builtin_amdgcn_readfirstlane(x)

#define KVSLEEP 6
#define CNTSLEEP 10
constexpr int HSW = 0;
#define NREP 8
#define OSLEEP 4
#define DPRIO 0
#define KVHG 7
#define UPROT 4
#define UPDEEP 0
#define QROT 1
#define SPLITBAR 0
#define GX 0
#define TCH2 1
#define DROT 1
#define HPLAIN 0
#define KDELAY 40
#define DDEPTH 4
#define DROTC 1
#define WPRE 0
#define WPACE 1
#define AMAP 1
#define SWAP 0
#define GLO 0
#define DBAR 4
#define QREG 1
#define RC 1
#define RT 1
#define RX 0
#define MPRE 0
#define ENDP 2
#define OLOOK 12
constexpr int F_ = 1792;
constexpr float QSCALE = 0.25f * 1.4426950408889634f;
constexpr unsigned SPIN_CAP = 1u << 20;

constexpr unsigned WS_KVF = 0;
constexpr unsigned WS_G  = 4096;
constexpr unsigned WS_HL = 16384;
constexpr unsigned WS_OL = WS_HL + 32768;
constexpr unsigned WS_KV = 131072;
constexpr unsigned WS_OF = 327680;
constexpr unsigned WS_HF = WS_OF + 2097152;
constexpr unsigned WS_DBG = 16u * 1024 * 1024;

#define STAMP(i)
#define CSTAMP(i)

DI f32x16 keep2(bf16x8 a, bf16x8 b, f32x16 c) { asm volatile("" :: "v"(a), "v"(b)); return c; }
DI unsigned pack_bf16(float lo, float hi) { f32x2 v = {lo, hi}; bf16x2_t r = __builtin_convertvector(v, bf16x2_t); return __builtin_bit_cast(unsigned, r); }
DI void glds(const char* sbase, unsigned voff, unsigned lds_base) {
    unsigned sv;
    asm volatile("s_mov_b32 %0, m0\n\ts_mov_b32 m0, %3\n\ts_nop 0\n\tglobal_load_lds_dwordx4 %1, %2\n\ts_mov_b32 m0, %0"
                 : "=&s"(sv) : "v"(voff), "s"(sbase), "s"(RFL(lds_base)) : "memory");
}
DI void glds_dev(const char* sbase, unsigned voff, unsigned lds_base) {
    unsigned sv;
    asm volatile("s_mov_b32 %0, m0\n\ts_mov_b32 m0, %3\n\ts_nop 0\n\tglobal_load_lds_dwordx4 %1, %2 sc1\n\ts_mov_b32 m0, %0"
                 : "=&s"(sv) : "v"(voff), "s"(sbase), "s"(RFL(lds_base)) : "memory");
}
DI u32x4 gld16_async(const char* sbase, unsigned voff) { u32x4 r; asm volatile("global_load_dwordx4 %0, %1, %2" : "=v"(r) : "v"(voff), "s"(sbase) : "memory"); return r; }
DI unsigned gld4_async(const char* sbase, unsigned voff) { unsigned r; asm volatile("global_load_dword %0, %1, %2" : "=v"(r) : "v"(voff), "s"(sbase) : "memory"); return r; }
DI void gst16_dev(char* sbase, unsigned voff, u32x4 v) { asm volatile("global_store_dwordx4 %0, %1, %2 sc1\n\ts_nop 1" :: "v"(voff), "v"(v), "s"(sbase) : "memory"); }
template <int N> DI void wait_vm_bar() { asm volatile("s_waitcnt vmcnt(%0) lgkmcnt(0)\n\ts_barrier" :: "i"(N) : "memory"); }
template <int N> DI void wait_vm_bar_lo() { asm volatile("s_setprio 0\n\ts_waitcnt vmcnt(%0) lgkmcnt(0)\n\ts_barrier\n\ts_setprio 3" :: "i"(N) : "memory"); }
DI void lds_bar_lo() { asm volatile("s_setprio 0\n\ts_waitcnt lgkmcnt(0)\n\ts_barrier\n\ts_setprio 3" ::: "memory"); }
DI void lds_bar() { asm volatile("s_waitcnt lgkmcnt(0)\n\ts_barrier" ::: "memory"); }
DI bf16x8 lds_b128(lds_cptr p) { return *(const __attribute__((address_space(3))) bf16x8*)p; }
DI f32x4 lds_f4(lds_cptr p) { return *(const __attribute__((address_space(3))) f32x4*)p; }
DI void lds_st_f4(lds_ptr p, f32x4 v) { *(__attribute__((address_space(3))) f32x4*)p = v; }
DI void lds_st_f(lds_ptr p, float v) { *(__attribute__((address_space(3))) float*)p = v; }
DI float lds_f(lds_cptr p) { return *(const __attribute__((address_space(3))) float*)p; }
DI s16x4 lds_tr(lds_cptr p) { return __builtin_bit_cast(s16x4, __builtin_amdgcn_ds_read_tr16_b64_v4i16((__attribute__((address_space(3))) v4i16_t*)p)); }
DI bf16x8 cat(s16x4 a, s16x4 b) { return __builtin_shufflevector(a, b, 0, 1, 2, 3, 4, 5, 6, 7); }
DI u32x4 pack8(const f32x16& x, int s, float sc) {
    u32x4 p;
    p[0] = pack_bf16(x[8 * s + 0] * sc, x[8 * s + 1] * sc); p[1] = pack_bf16(x[8 * s + 2] * sc, x[8 * s + 3] * sc);
    p[2] = pack_bf16(x[8 * s + 4] * sc, x[8 * s + 5] * sc); p[3] = pack_bf16(x[8 * s + 6] * sc, x[8 * s + 7] * sc);
    return p;
}
DI u32x4 pack44(f32x4 a, f32x4 b, float sc) { u32x4 p; p[0] = pack_bf16(a[0] * sc, a[1] * sc); p[1] = pack_bf16(a[2] * sc, a[3] * sc); p[2] = pack_bf16(b[0] * sc, b[1] * sc); p[3] = pack_bf16(b[2] * sc, b[3] * sc); return p; }

#define PRIO_HI() __builtin_amdgcn_s_setprio(3)
#define PRIO_LO() __builtin_amdgcn_s_setprio(GLO)
template <int SLP> DI void line_wait(char* line, int n, unsigned long long tag, int lane) {
    const unsigned off = (lane < n ? lane : 0) * 8;
#pragma unroll 1
    for (unsigned s = 0; s < SPIN_CAP; ++s) {
        unsigned long long v;
        asm volatile("global_load_dwordx2 %0, %1, %2 sc1\n\ts_setprio 0\n\ts_waitcnt vmcnt(0)\n\ts_setprio 3" : "=v"(v) : "v"(off), "s"(line) : "memory");
        if (__all(v == tag)) break;
        PRIO_LO(); __builtin_amdgcn_s_sleep(SLP); PRIO_HI();
    }
}

struct T64 { unsigned xs, ws; int xoff[2], woff[2][2]; };
template <int KTOT, bool XFRAG, bool PERMN>
DI T64 t64_setup(int tok0, int n0, int w, int lane) {
    T64 t;
    if (XFRAG) t.xs = (((tok0 >> 5) + (w >> 2)) * (KTOT / 16) + (w & 3)) * 1024 + lane * 16;
    else { int row = 8 * w + (lane >> 3); int ch = (lane & 7) ^ ((row >> 1) & 7); t.xs = (tok0 + row) * 1024 + ch * 16; }
    { int row = 8 * w + (lane >> 3); int ch = (lane & 7) ^ (((row >> 1) & 1) << 2); t.ws = row * 1024 + n0 * 2 + ch * 16; }
    const int ti = w & 1, ni = (w >> 1) & 1, kh = w >> 2, h = lane >> 5, r32 = lane & 31;
#pragma unroll
    for (int i = 0; i < 2; ++i) {
        const int ks = 2 * kh + i;
        if (XFRAG) t.xoff[i] = (ti * 4 + ks) * 1024 + lane * 16;
        else { int tok = ti * 32 + r32; t.xoff[i] = tok * 128 + (((2 * ks + h) ^ ((tok >> 1) & 7)) * 16); }
        const int g = (lane >> 4) & 1, q = (lane & 15) >> 2, p = lane & 3;
        const int pp = PERMN ? (((p & 1) << 1) | (p >> 1)) : p;
        const int col = ni * 32 + g * 16 + 4 * pp;
#pragma unroll
        for (int tt = 0; tt < 2; ++tt) {
            const int row = XFRAG ? (16 * ks + 8 * tt + 4 * h + q) : (16 * ks + 8 * h + 4 * tt + q);
            t.woff[i][tt] = row * 128 + (((col >> 3) ^ (((row >> 1) & 1) << 2)) * 16) + (col & 7) * 2;
        }
    }
    return t;
}
template <bool NOM = false> DI void t64_compute(const T64& t, lds_cptr b, f32x16& acc) {
#pragma unroll
    for (int i = 0; i < 2; ++i) {
        bf16x8 xf = lds_b128(b + t.xoff[i]);
        bf16x8 wf = cat(lds_tr(b + 8192 + t.woff[i][0]), lds_tr(b + 8192 + t.woff[i][1]));
        if (NOM) asm volatile("" :: "v"(wf), "v"(xf)); else acc = MFMA(wf, xf, acc);
    }
}
DI void t64_xchg_write(const f32x16& acc, lds_ptr scr, int w, int lane) {
    const int kh = w >> 2;
    f32x4 a = kh ? (f32x4){acc[0], acc[1], acc[2], acc[3]} : (f32x4){acc[8], acc[9], acc[10], acc[11]};
    f32x4 b = kh ? (f32x4){acc[4], acc[5], acc[6], acc[7]} : (f32x4){acc[12], acc[13], acc[14], acc[15]};
    lds_st_f4(scr + w * 2048 + lane * 16, a); lds_st_f4(scr + w * 2048 + 1024 + lane * 16, b);
}
DI void t64_xchg_read(const f32x16& acc, lds_cptr scr, int w, int lane, f32x4& r0, f32x4& r1) {
    const int kh = w >> 2;
    r0 = kh ? (f32x4){acc[8], acc[9], acc[10], acc[11]} : (f32x4){acc[0], acc[1], acc[2], acc[3]};
    r1 = kh ? (f32x4){acc[12], acc[13], acc[14], acc[15]} : (f32x4){acc[4], acc[5], acc[6], acc[7]};
    r0 += lds_f4(scr + (w ^ 4) * 2048 + lane * 16); r1 += lds_f4(scr + (w ^ 4) * 2048 + 1024 + lane * 16);
}

DI void kv_job(int tile, const char* x, const char* wk, const char* wv, const char* gw, char* ws, char* lds, int w, int lane, unsigned long long tag) {
    PRIO_HI();
    constexpr int WB = 8192;
    const int h = lane >> 5, r32 = lane & 31;
    const unsigned sb = (unsigned)(uintptr_t)lds + w * WB;
    { const int row = lane >> 3; const unsigned xo = (tile * 32 + row) * 1024 + w * 128;
      const unsigned xe = xo + (((lane & 7) ^ (row >> 1)) * 16), xd = xo + (((lane & 7) ^ (4 + (row >> 1))) * 16);
      const unsigned wo_ = w * 2048 + lane * 16;
#pragma unroll
      for (int i = 0; i < 4; ++i) glds(x + i * 8192, (i & 1) ? xd : xe, sb + i * 1024);
      glds(wv, wo_, sb + 4096); glds(wv + 1024, wo_, sb + 5120); glds(wk, wo_, sb + 6144); glds(wk + 1024, wo_, sb + 7168);
      glds(gw, lane * 16, (unsigned)(uintptr_t)lds + 65536 + w * 1024); }
    STAMP(11);
    asm volatile("s_waitcnt vmcnt(0)" ::: "memory");
    f32x16 a = {}, ga = {};
    const lds_cptr xb = (lds_cptr)lds + w * WB + r32 * 128;
    const lds_cptr wb = (lds_cptr)lds + w * WB + 4096 + ((lane >> 4) & 1) * 2048 + (8 * h + ((lane & 15) >> 2)) * 32 + (lane & 3) * 8;
    const lds_cptr gb = (lds_cptr)lds + 65536 + w * 1152 + h * 16;
#pragma unroll
    for (int ks = 0; ks < 4; ++ks) {
        const bf16x8 xf = lds_b128(xb + (((2 * ks + h) ^ ((r32 >> 1) & 7)) * 16));
        a = MFMA(cat(lds_tr(wb + ks * 512), lds_tr(wb + ks * 512 + 128)), xf, a); ga = MFMA(lds_b128(gb + ks * 32), xf, ga);
    }
    STAMP(12);
    { lds_ptr p = (lds_ptr)lds + w * WB;
      lds_st_f4(p + lane * 16, (f32x4){a[8], a[9], a[10], a[11]}); lds_st_f4(p + 1024 + lane * 16, (f32x4){a[12], a[13], a[14], a[15]});
#pragma unroll
      for (int r = 0; r < 8; ++r) lds_st_f(p + 2048 + ((r & 3) + 8 * (r >> 2) + 4 * h) * 144 + r32 * 4, a[r]);
      lds_st_f(p + 4352 + lane * 4, ga[0]); }
    lds_bar();
    if (w < 2) {
        f32x4 s0 = {}, s1 = {};
        const lds_cptr p = (lds_cptr)lds + (w == 0 ? lane * 16 : 2048 + (lane & 15) * 144 + ((lane >> 4) & 1) * 16 + h * 64);
        const int d1 = w == 0 ? 1024 : 32;
#pragma unroll
        for (int q = 0; q < 8; ++q) { s0 += lds_f4(p + q * WB); s1 += lds_f4(p + q * WB + d1); }
        gst16_dev(ws + WS_KV + tile * 2048 + w * 1024, lane * 16, pack44(s0, s1, 1.0f));
        STAMP(13);
    } else if (w == 2) {
        float s = 0.f;
#pragma unroll
        for (int q = 0; q < 8; ++q) s += lds_f((lds_cptr)lds + q * WB + 4352 + lane * 4);
        if (lane < 32) __hip_atomic_store((gu32*)(ws + WS_G) + tile * 32 + lane, __float_as_uint(1.0f / (1.0f + __expf(-s))), RLX_AGENT);
    }
    wait_vm_bar<0>();
    if (w == HSW && lane < NREP) __hip_atomic_store((gu64*)(ws + WS_KVF) + lane * 64 + tile, tag, RLX_AGENT);
    STAMP(14);
    PRIO_LO();
}

template <bool HALF> DI void up_job(int rp, int cp, int half, const char* x, const char* wg, const char* wu, char* ws, char* lds, int w, int lane, unsigned long long tag) {
    constexpr int STB = 24576, NST = 16;
    const unsigned lds0 = (unsigned)(uintptr_t)lds + w * 1024;
    unsigned xs, gs;
    { int row = 16 * w + (lane >> 2); int c = (lane & 3) ^ ((row >> 2) & 3); xs = (rp * 128 + (HALF ? half * 64 + (row & 63) : row)) * 1024 + c * 16; }
    { int row = 4 * w + (lane >> 4); int c = (lane & 15) ^ ((row & 3) << 2); gs = row * (F_ * 2) + cp * 256 + c * 16; }
    int rot = UPROT * (rp & 3);
#define UP_STAGE(s) do { const unsigned b_ = lds0 + ((s) % 3) * STB; const int k_ = ((s) + rot) & 15; glds(x + k_ * 64, xs, b_); glds(wg + k_ * (32 * F_ * 2), gs, b_ + 8192); glds(wu + k_ * (32 * F_ * 2), gs, b_ + 16384); } while (0)
    UP_STAGE(0); UP_STAGE(1);
    const int th = w & 1, fg = w >> 1, h = lane >> 5, r32 = lane & 31;
    lds_cptr xb[2], wb;
#pragma unroll
    for (int ks = 0; ks < 2; ++ks) { int tok = th * (HALF ? 32 : 64) + r32; xb[ks] = (lds_cptr)lds + tok * 64 + (((2 * ks + h) ^ ((tok >> 2) & 3)) * 16); }
    { const int g = (lane >> 4) & 1, q = (lane & 15) >> 2, p = lane & 3; const int col = fg * 32 + g * 16 + 4 * p;
      wb = (lds_cptr)lds + 8192 + (8 * h + q) * 256 + (((col >> 3) ^ (q << 2)) * 16) + (col & 7) * 2; }
    f32x16 ag[2] = {}, au[2] = {};
    unsigned g0, g1, goff = (rp * 128 + (HALF ? half * 64 + th * 32 : th * 64) + r32) * 4;
    asm volatile("" : "+v"(goff));
#define SBAR() __builtin_amdgcn_sched_barrier(0)
#define UP_RD(F, so, ks) do { F[0] = cat(lds_tr(wb + (so) + (ks) * 4096), lds_tr(wb + (so) + (ks) * 4096 + 1024)); F[1] = lds_b128(xb[ks] + (so)); if (!HALF) F[2] = lds_b128(xb[ks] + (so) + 2048); \
        F[3] = cat(lds_tr(wb + (so) + 8192 + (ks) * 4096), lds_tr(wb + (so) + 8192 + (ks) * 4096 + 1024)); } while (0)
    bf16x8 fa[4], fb[4];
#pragma unroll
    for (int s = 0; s < NST; ++s) {
        if (s + 1 < NST) wait_vm_bar<3>(); else wait_vm_bar<0>();
        if (s == 4) PRIO_LO();
        const int so = (s % 3) * STB;
        SBAR(); UP_RD(fb, so, 0); SBAR();
        asm volatile("" : "+s"(rot));
        const unsigned b_ = lds0 + ((s + 2) % 3) * STB; const int k_ = (s + 2 + rot) & 15;
        if (s > 0) { ag[0] = UMFMA(fa[0], fa[1], ag[0]); asm volatile("" : "+v"(ag[0])); }
        SBAR(); if (s + 2 < NST && !(GX & 8)) glds(x + k_ * 64, xs, b_); SBAR();
        if (s > 0 && !HALF) { ag[1] = UMFMA(fa[0], fa[2], ag[1]); asm volatile("" : "+v"(ag[1])); }
        SBAR(); if (s + 2 < NST && !(GX & 8)) glds(wg + k_ * (32 * F_ * 2), gs, b_ + 8192); SBAR();
        if (s > 0) { au[0] = UMFMA(fa[3], fa[1], au[0]); asm volatile("" : "+v"(au[0])); }
        SBAR(); if (s + 2 < NST && !(GX & 8)) glds(wu + k_ * (32 * F_ * 2), gs, b_ + 16384); SBAR();
        if (s > 0 && !HALF) { au[1] = UMFMA(fa[3], fa[2], au[1]); asm volatile("" : "+v"(au[1])); }
        SBAR();
        if (s == NST - 3 && w == HSW) { PRIO_HI(); line_wait<CNTSLEEP>(ws + WS_KVF + (blockIdx.x & (NREP - 1)) * 512 + rp * 32, 4, tag, lane); PRIO_LO(); }
        if (s == NST - 2) asm volatile("global_load_dword %0, %2, %3 sc1\n\tglobal_load_dword %1, %2, %3 offset:128 sc1" : "=&v"(g0), "=&v"(g1) : "v"(goff), "s"(ws + WS_G) : "memory");
        UP_RD(fa, so, 1); SBAR();
        ag[0] = UMFMA(fb[0], fb[1], ag[0]); au[0] = UMFMA(fb[3], fb[1], au[0]);
        if (!HALF) { ag[1] = UMFMA(fb[0], fb[2], ag[1]); au[1] = UMFMA(fb[3], fb[2], au[1]); }
        asm volatile("" : "+v"(ag[0]), "+v"(ag[1]), "+v"(au[0]), "+v"(au[1])); SBAR();
    }
    ag[0] = UMFMA(fa[0], fa[1], ag[0]); au[0] = UMFMA(fa[3], fa[1], au[0]);
    if (!HALF) { ag[1] = UMFMA(fa[0], fa[2], ag[1]); au[1] = UMFMA(fa[3], fa[2], au[1]); }
#undef UP_RD
#undef UP_STAGE
    PRIO_HI();
    STAMP(2);
    asm volatile("" : "+v"(g0), "+v"(g1));
#pragma unroll
    for (int i = 0; i < (HALF ? 1 : 2); ++i) {
        const float sc = 1.0f - __uint_as_float(i ? g1 : g0);
        f32x16 hh = ag[i] * au[i];
        char* dst = ws + WS_HF + ((rp * 4 + (HALF ? half * 2 + th : th * 2 + i)) * 112 + cp * 8 + fg * 2) * 1024;
        gst16_dev(dst, lane * 16, pack8(hh, 0, sc));
        gst16_dev(dst, 1024 + lane * 16, pack8(hh, 1, sc));
    }
    wait_vm_bar_lo<0>();
    if (w == HSW && lane < (HALF ? 8 : 16)) __hip_atomic_store((gu64*)(ws + WS_HL + (rp * 16 + (HALF ? half * 8 : 0) + lane) * 128) + cp, tag, RLX_AGENT);
}

DI u32x4 gld16_dev(const char* sbase, unsigned voff) { u32x4 r; asm volatile("global_load_dwordx4 %0, %1, %2 sc1" : "=v"(r) : "v"(voff), "s"(sbase) : "memory"); return r; }
DI unsigned flag_mask(const char* line, unsigned off, unsigned long long tag) {
    unsigned long long v;
    asm volatile("global_load_dwordx2 %0, %1, %2 sc1\n\ts_waitcnt vmcnt(0)\n\ts_setprio 3" : "=v"(v) : "v"(off), "s"(line) : "memory");
    const unsigned m = (unsigned)__ballot(v == tag);
    PRIO_LO();
    return m;
}
DI void down_out_job(int tp, int cg, const char* wd, const char* wo, char* ws, char* out, char* lds, int w, int lane, unsigned long long tag) {
    constexpr int D = DDEPTH;
    const unsigned lds0 = (unsigned)(uintptr_t)lds;
    const int h = lane >> 5;
    const unsigned l16 = lane * 16;
    unsigned wsrc; { const int row = lane >> 3; wsrc = row * 1024 + cg * 128 + (((lane & 7) ^ (((row >> 1) & 1) << 2)) * 16); }
    int woff[2];
    { const int g = (lane >> 4) & 1, q = (lane & 15) >> 2, p = lane & 3, pp = ((p & 1) << 1) | (p >> 1), row = 4 * h + q;
#pragma unroll
      for (int ni = 0; ni < 2; ++ni) { const int col = ni * 32 + g * 16 + 4 * pp; woff[ni] = row * 128 + (((col >> 3) ^ (((row >> 1) & 1) << 2)) * 16) + (col & 7) * 2; } }
    const char* hx = ws + WS_HF + (tp * 224 + w) * 1024;
    const char* wdw = wd + w * 16384;
    char* hl = ws + WS_HL + (tp * 8 + cg) * 128;
    const unsigned ring = RFL(lds0 + w * 10240);
    const lds_cptr ringp = (lds_cptr)lds + w * 10240;
    int rot = RFL((RC * cg * 7 / 4 + RT * (tp & 3) * 7 / 2 + RX * (tp >> 2) * 7 / 4) % 14);
    f32x16 acc[4] = {};
    u32x4 xr[D][2];
    char* ol = ws + WS_OL + (tp * 8 + cg) * 128 + w * 8;
    unsigned long long ofl; unsigned zero = 0; asm volatile("" : "+v"(zero));
    asm volatile("" : "+v"(acc[0]), "+v"(acc[1]), "+v"(acc[2]), "+v"(acc[3]));
    if (w == HSW) { line_wait<CNTSLEEP>(hl, 14, tag, lane); STAMP(12); }
    wait_vm_bar_lo<0>();
    STAMP(3);
    PRIO_LO();
#define DN_ISSUE(i) do { \
        asm volatile("" : "+s"(rot)); \
        const int p_ = (i) + rot >= 14 ? (i) + rot - 14 : (i) + rot; \
        xr[(i) % D][0] = gld16_dev(hx + p_ * 8192, l16); xr[(i) % D][1] = gld16_dev(hx + p_ * 8192 + 114688, l16); \
        glds(wdw + p_ * 131072, wsrc, ring + ((i) % 5) * 2048); glds(wdw + p_ * 131072 + 8192, wsrc, ring + ((i) % 5) * 2048 + 1024); } while (0)
#define WO_ISSUE(j) do { glds(wo + (w * 4 + (j)) * 16384, wsrc, ring + (((j) + 14) % 5) * 2048); glds(wo + (w * 4 + (j)) * 16384 + 8192, wsrc, ring + (((j) + 14) % 5) * 2048 + 1024); } while (0)
#pragma unroll
    for (int i = 0; i < D; ++i) DN_ISSUE(i);
#pragma unroll
    for (int i = 0; i < 14; ++i) {
        constexpr int dummy = 0; (void)dummy;
        const int full = (i + D - 1 < 14 ? D - 1 : 13 - i), half = D - 1 - full;
        asm volatile("s_waitcnt vmcnt(%2)" : "+v"(xr[i % D][0]), "+v"(xr[i % D][1]) : "i"(4 * full + 2 * half) : "memory");
        const lds_cptr rb = ringp + (i % 5) * 2048;
        const bf16x8 w0 = cat(lds_tr(rb + woff[0]), lds_tr(rb + woff[0] + 1024)), w1 = cat(lds_tr(rb + woff[1]), lds_tr(rb + woff[1] + 1024));
        const bf16x8 x0 = __builtin_bit_cast(bf16x8, xr[i % D][0]), x1 = __builtin_bit_cast(bf16x8, xr[i % D][1]);
        acc[0] = MFMA(w0, x0, acc[0]); acc[1] = MFMA(w0, x1, acc[1]); acc[2] = MFMA(w1, x0, acc[2]); acc[3] = MFMA(w1, x1, acc[3]);
        asm volatile("" : "+v"(acc[0]), "+v"(acc[1]), "+v"(acc[2]), "+v"(acc[3]));
        if (i + D < 14) DN_ISSUE(i + D); else WO_ISSUE(i + D - 14);
        if (i == OLOOK) asm volatile("global_load_dwordx2 %0, %1, %2 sc1" : "=v"(ofl) : "v"(zero), "s"(ol) : "memory");
        if (DBAR && i % DBAR == DBAR - 1 && i < 12) __builtin_amdgcn_s_barrier();
    }
#pragma unroll
    for (int j = D; j < 4; ++j) WO_ISSUE(j);
#undef DN_ISSUE
#undef WO_ISSUE
    STAMP(4);
    PRIO_HI();
    STAMP(6);
    asm volatile("s_waitcnt vmcnt(%1)" : "+v"(ofl) : "i"(2 * (13 - OLOOK)) : "memory");
    if (!__all(ofl == tag)) line_wait<OSLEEP>(ol, 1, tag, lane);
    STAMP(5);
    const char* ox = ws + WS_OF + (tp * 64 + w * 4) * 1024;
    u32x4 of[4][2];
#pragma unroll
    for (int i = 0; i < 4; ++i) { of[i][0] = gld16_dev(ox + i * 1024, l16); of[i][1] = gld16_dev(ox + i * 1024 + 32768, l16); }
#pragma unroll
    for (int i = 0; i < 4; ++i) {
        const lds_cptr rb = ringp + ((i + 14) % 5) * 2048;
        if (i == 0) asm volatile("s_waitcnt vmcnt(8)" ::: "memory");
        const bf16x8 w0 = cat(lds_tr(rb + woff[0]), lds_tr(rb + woff[0] + 1024)), w1 = cat(lds_tr(rb + woff[1]), lds_tr(rb + woff[1] + 1024));
        asm volatile("s_waitcnt vmcnt(%2)" : "+v"(of[i][0]), "+v"(of[i][1]) : "i"(6 - 2 * i) : "memory");
        const bf16x8 x0 = __builtin_bit_cast(bf16x8, of[i][0]), x1 = __builtin_bit_cast(bf16x8, of[i][1]);
        acc[0] = MFMA(w0, x0, acc[0]); acc[1] = MFMA(w0, x1, acc[1]); acc[2] = MFMA(w1, x0, acc[2]); acc[3] = MFMA(w1, x1, acc[3]);
    }
    f32x4 bq[2];
#pragma unroll
    for (int r = 0; r < 2; ++r) {
#pragma unroll
        for (int j = 0; j < 8; ++j) { const f32x16& a = acc[j & 3]; const int o = (j >> 2) * 8 + r * 4;
            lds_st_f4((lds_ptr)lds + (w * 10 + j) * 1024 + l16, (f32x4){a[o], a[o + 1], a[o + 2], a[o + 3]}); }
        lds_bar_lo();
        f32x4 sum = lds_f4((lds_cptr)lds + w * 1024 + l16);
#pragma unroll
        for (int q = 1; q < 8; ++q) sum += lds_f4((lds_cptr)lds + (q * 10 + w) * 1024 + l16);
        bq[r] = sum;
        if (r == 0) { asm volatile("" : "+v"(bq[0])); lds_bar(); }
    }
    const int tok = tp * 64 + (w & 1) * 32 + (lane & 31);
    *(u32x4*)(out + (size_t)tok * 1024 + (cg * 64 + ((w >> 1) & 1) * 32 + (w >> 2) * 16 + 8 * h) * 2) = pack44(bq[0], bq[1], 1.0f);
}

constexpr int KVT = 2080;
DI float xhalf_max(float m) { auto rr = __builtin_amdgcn_permlane32_swap(__float_as_uint(m), __float_as_uint(m), false, false); return fmaxf(__uint_as_float(rr[0]), __uint_as_float(rr[1])); }
DI float max16(const f32x16& s) {
    float a = fmaxf(fmaxf(s[0], s[1]), s[2]), b = fmaxf(fmaxf(s[3], s[4]), s[5]);
    a = fmaxf(fmaxf(a, s[6]), s[7]); b = fmaxf(fmaxf(b, s[8]), s[9]); a = fmaxf(fmaxf(a, s[10]), s[11]); b = fmaxf(fmaxf(b, s[12]), s[13]);
    a = fmaxf(fmaxf(a, s[14]), s[15]); return fmaxf(a, b);
}
DI u32x4 exp_pack(const f32x16& s, int o) {
    u32x4 p;
#pragma unroll
    for (int i = 0; i < 4; ++i) p[i] = pack_bf16(__builtin_amdgcn_exp2f(s[o + 2 * i]), __builtin_amdgcn_exp2f(s[o + 2 * i + 1]));
    return p;
}
DI f32x16 attn_exact(const char* kv, bf16x8 qf, int lane) {
    f32x16 o = {}; float m = -INFINITY;
#pragma unroll 1
    for (int t = 0; t < 64; ++t) {
        u32x4 kf, v0, v1;
        asm volatile("global_load_dwordx4 %0, %3, %5 sc1\n\tglobal_load_dwordx4 %1, %4, %5 offset:1024 sc1\n\tglobal_load_dwordx4 %2, %4, %5 offset:1536 sc1\n\ts_waitcnt vmcnt(0)"
                     : "=&v"(kf), "=&v"(v0), "=&v"(v1) : "v"(t * 2048 + lane * 16), "v"(t * 2048 + (lane >> 5) * 256 + (lane & 15) * 16), "s"(kv) : "memory");
        if (lane & 16) { const unsigned c = (lane & 11) == 0 ? 0x3F803F80u : 0u; v0 = (u32x4){c, c, c, c}; v1 = v0; }
        f32x16 s = MFMA(__builtin_bit_cast(bf16x8, kf), qf, (f32x16){});
        const float mn = fmaxf(m, xhalf_max(max16(s)));
        const float al = __builtin_amdgcn_exp2f(m - mn);
        m = mn;
#pragma unroll
        for (int i = 0; i < 16; ++i) { o[i] *= al; s[i] -= mn; }
        o = MFMA(__builtin_bit_cast(bf16x8, v0), __builtin_bit_cast(bf16x8, exp_pack(s, 0)), o);
        o = MFMA(__builtin_bit_cast(bf16x8, v1), __builtin_bit_cast(bf16x8, exp_pack(s, 8)), o);
    }
    return o;
}
#define A_E(r) "v_exp_f32 v" #r ", v" #r "\n\t"
#define A_C(d, a, b) "v_cvt_pk_bf16_f32 v" #d ", v" #a ", v" #b "\n\t"
#define A_QK(d, q) "v_mfma_f32_32x32x16_bf16 v[" d "], v[100:103], v[" q "], v[80:95]\n\t"
#define A_PV(o, a, p) "v_mfma_f32_32x32x16_bf16 v[" o "], v[" a "], v[" p "], v[" o "]\n\t"
#define A_W(n) "s_waitcnt lgkmcnt(" #n ")\n\t"
#define A_RD(d, base, u) "ds_read_b128 v[" d "], " base " offset:((" u ")%%16)*4160\n\t"
#define A_U(S0,S1,S2,S3,S4,S5,S6,S7,S8,S9,S10,S11,S12,S13,S14,S15,P14,P15, M1, M2, M3) \
    A_E(S0) A_C(119,P14,P15) A_E(S1) A_E(S2) M1 A_E(S3) A_C(112,S0,S1) A_E(S4) A_E(S5) A_C(113,S2,S3) \
    M2 A_E(S6) A_E(S7) A_C(114,S4,S5) A_E(S8) A_E(S9) A_C(115,S6,S7) A_E(S10) A_E(S11) \
    M3 A_C(116,S8,S9) A_E(S12) A_E(S13) A_C(117,S10,S11) A_E(S14) A_E(S15) A_C(118,S12,S13)
#define A_TA(U) A_U(48,49,50,51,52,53,54,55,56,57,58,59,60,61,62,63,78,79, \
    A_PV("16:31","108:111","116:119") A_RD("108:111", "%11", U), A_QK("64:79","12:15") A_RD("100:103", "%9", U "+1"), A_W(2) A_PV("32:47","104:107","112:115"))
#define A_TB(U) A_U(64,65,66,67,68,69,70,71,72,73,74,75,76,77,78,79,62,63, \
    A_W(0) A_PV("32:47","108:111","116:119"), A_QK("48:63","96:99"), A_PV("16:31","104:107","112:115") A_RD("104:107", "%10", U "+1"))
#define A_OPS "+{v[32:47]}"(oA), "+{v[16:31]}"(oB), "+{v[48:63]}"(sA), "+{v[64:79]}"(sB), "+{v[100:103]}"(kn), "+{v[104:107]}"(v0), "+{v[108:111]}"(v1), "+{v[112:115]}"(p0), "+{v[116:119]}"(p1)
#define A_CHUNKP(C, BAR, G0, G1, P0, P1, P2, P3, P4, P5, P6, P7) \
    asm volatile(P0 A_TA(#C "*4") P1 A_TB(#C "*4") P2 A_TA(#C "*4+1") BAR P3 A_TB(#C "*4+1") G0 P4 A_TA(#C "*4+2") G1 P5 A_TB(#C "*4+2") P6 A_TA(#C "*4+3") P7 A_TB(#C "*4+3") \
                 : A_OPS : "v"(bk), "v"(bv0), "v"(bv1), "v"(kvo), "s"(kv + ((C) + 3) * 16384), "s"(dst0 + (((C) + 3) & 3) * 8 * KVT), "{v[80:95]}"(cn), "{v[96:99]}"(qA), "{v[12:15]}"(qB) : "memory")
#define A_CHUNK(C, BAR, G0, G1) A_CHUNKP(C, BAR, G0, G1, A_P0, A_P1, A_P2, A_P3, A_P4, A_P5, A_P6, A_P7)
#define APRIO 2
#define SP(n) "s_setprio " #n "\n\t"
#define A_P0 ""
#define A_P1 ""
#define A_P2 SP(0)
#define A_P3 SP(2)
#define A_P4 ""
#define A_P5 ""
#define A_P6 ""
#define A_P7 SP(1)
#define LASTBAR ""
#define A_BAR(n) "s_waitcnt vmcnt(" #n ")\n\ts_barrier\n\t"
#define A_G0 "s_mov_b32 m0, %14\n\ts_nop 0\n\tglobal_load_lds_dwordx4 %12, %13 sc1\n\t"
#define A_G1 "global_load_lds_dwordx4 %12, %13 offset:1024 sc1\n\t"
DI void attn_role(int blk, const char* x, const char* wq, char* ws, char* lds, int w, int lane, unsigned long long tag) {
    const int xcd = blk & 7;
    const int tp = (xcd >> 1) * 8 + ((blk >> 3) & 7), hg = (xcd & 1) * 4 + (blk >> 6);
    const unsigned lds0 = (unsigned)(uintptr_t)lds;
    const T64 t = t64_setup<512, false, false>(tp * 64, hg * 64, w, lane);
    const int qrot = QROT * (2 * (hg & 3) + tp);
#define Q_STAGE(s) do { const unsigned b_ = lds0 + w * 1024 + (((s) + 4) % 5) * 16384; const int k_ = ((s) + qrot) & 7; glds(x + k_ * 128, t.xs, b_); glds(wq + k_ * 65536, t.ws, b_ + 8192); } while (0)
#define Q_COMP(s) t64_compute(t, (lds_cptr)lds + (((s) + 4) % 5) * 16384, acc)
    if (blk >= 192) __builtin_amdgcn_s_sleep(KDELAY);
    Q_STAGE(0); Q_STAGE(1); Q_STAGE(2); Q_STAGE(3); Q_STAGE(4);
    u32x4 qr[3][2];
#pragma unroll
    for (int j = 0; j < 3; ++j) { const int k_ = (5 + j + qrot) & 7; qr[j][0] = gld16_async(x + k_ * 128, t.xs); qr[j][1] = gld16_async(wq + k_ * 65536, t.ws); }
#define Q_PUT(j) do { lds_ptr p_ = (lds_ptr)lds + w * 1024 + lane * 16 + (((j) + 9) % 5) * 16384; \
        *(__attribute__((address_space(3))) u32x4*)p_ = qr[j][0]; *(__attribute__((address_space(3))) u32x4*)(p_ + 8192) = qr[j][1]; } while (0)
    f32x16 acc = {};
    STAMP(9);
    wait_vm_bar<12>(); STAMP(10);
    Q_COMP(0); Q_COMP(1);
    wait_vm_bar<6>();
    Q_COMP(2);
    asm volatile("s_waitcnt vmcnt(2)" : "+v"(qr[0][0]), "+v"(qr[0][1]), "+v"(qr[1][0]), "+v"(qr[1][1]) :: "memory");
    Q_PUT(0); Q_PUT(1);
    Q_COMP(3); Q_COMP(4);
    asm volatile("s_waitcnt vmcnt(0) lgkmcnt(0)\n\ts_barrier" : "+v"(qr[2][0]), "+v"(qr[2][1]) :: "memory");
    Q_PUT(2);
    Q_COMP(5); Q_COMP(6);
    lds_bar();
    Q_COMP(7);
#undef Q_PUT
#undef Q_STAGE
#undef Q_COMP
#pragma unroll
    for (int i = 0; i < 4; ++i) lds_st_f4((lds_ptr)lds + 32768 + w * 4096 + i * 1024 + lane * 16, (f32x4){acc[4 * i], acc[4 * i + 1], acc[4 * i + 2], acc[4 * i + 3]});
    lds_bar();
    const int kh = w >> 2;
#pragma unroll
    for (int i = 0; i < 4; ++i) { const f32x4 v = lds_f4((lds_cptr)lds + 32768 + (w ^ 4) * 4096 + i * 1024 + lane * 16);
#pragma unroll
        for (int j = 0; j < 4; ++j) acc[4 * i + j] += v[j]; }
    const u32x4 q0 = pack8(acc, 0, QSCALE), q1 = pack8(acc, 1, QSCALE);
    bf16x8 qA = __builtin_bit_cast(bf16x8, kh ? q0 : q1), qB = __builtin_bit_cast(bf16x8, kh ? q1 : q0);
    const char* kv = ws + WS_KV;
    unsigned kvo = w * 2048 + lane * 16, goff = (tp * 64 + (w & 1) * 32 + (lane & 31)) * 4;
    const unsigned dst0 = RFL(lds0 + w * KVT);
    unsigned bk = lds0 + kh * KVT + lane * 16;
    const unsigned pad = lds0 + 2048 + ((lane & 11) == 0 ? 0 : 16);
    unsigned bv0 = (lane & 16) ? pad : lds0 + kh * KVT + 1024 + (lane >> 5) * 256 + (lane & 15) * 16;
    unsigned bv1 = (lane & 16) ? pad : bv0 + 512;
    f32x16 oA = {}, oB = {}, sB = {};
    u32x4 p0 = {0u, 0u, 0u, 0u}, p1 = {0u, 0u, 0u, 0u};
    bf16x8 v1 = {};
    asm volatile("" : "+{v[96:99]}"(qA), "+{v[12:15]}"(qB), "+v"(kvo), "+v"(goff), "+v"(bk), "+v"(bv0), "+v"(bv1), "+{v[32:47]}"(oA), "+{v[16:31]}"(oB), "+{v[112:115]}"(p0), "+{v[116:119]}"(p1), "+{v[108:111]}"(v1));
    STAMP(1);
    if (w == HSW) line_wait<KVSLEEP>(ws + WS_KVF + (blk & (NREP - 1)) * 512, 64, tag, lane);
    __builtin_amdgcn_s_setprio(SWAP ? 2 : 0);
    lds_bar();
    STAMP(2);
#pragma unroll
    for (int c = 0; c < 4; ++c) { glds_dev(kv + c * 16384, kvo, dst0 + c * 8 * KVT); glds_dev(kv + c * 16384 + 1024, kvo, dst0 + c * 8 * KVT + 1024); }
    unsigned gu; asm volatile("global_load_dword %0, %1, %2 sc1" : "=v"(gu) : "v"(goff), "s"(ws + WS_G) : "memory");
    if (w == 1) { const unsigned c = (lane & 1) ? 0u : 0x3F803F80u; *(__attribute__((address_space(3))) u32x4*)((lds_ptr)lds + (lane >> 1) * KVT + 2048 + (lane & 1) * 16) = (u32x4){c, c, c, c}; }
    wait_vm_bar<7>();
    STAMP(3); CSTAMP(5);
    f32x16 cn, sA;
    bf16x8 kn = lds_b128((lds_cptr)(uintptr_t)bk);
    bf16x8 v0 = lds_b128((lds_cptr)(uintptr_t)bv0);
    { const bf16x8 k0 = lds_b128((lds_cptr)lds + lane * 16);
      sA = MFMA(k0, qA, (f32x16){}); sB = MFMA(k0, qB, (f32x16){});
      const float m = xhalf_max(fmaxf(max16(sA), max16(sB)));
#pragma unroll
      for (int i = 0; i < 16; ++i) cn[i] = -m; }
    sA = MFMA(kn, qA, cn); sB[14] = 0.f; sB[15] = 0.f;
    unsigned m0save; asm volatile("s_mov_b32 %0, m0\n\ts_nop 15" : "=s"(m0save), A_OPS);
    A_CHUNK(0, A_BAR(5), "", "");
    A_CHUNK(1, A_BAR(3), A_G0, A_G1);
    A_CHUNK(2, A_BAR(2), A_G0, A_G1);
    A_CHUNK(3, A_BAR(2), A_G0, A_G1);
    A_CHUNK(4, A_BAR(2), A_G0, A_G1);
    A_CHUNK(5, A_BAR(2), "", "");
    A_CHUNK(6, A_BAR(0), "", "");
    A_CHUNKP(7, "s_barrier\n\t", "", "", A_P0, A_P1, A_P2, SP(2), "", SP(1), "", SP(0));
    asm volatile("s_mov_b32 m0, %1" : "+v"(gu) : "s"(m0save));
    asm volatile("s_setprio 3\n\t" A_C(119,78,79) "s_waitcnt lgkmcnt(0)\n\ts_nop 1\n\t" A_PV("16:31","108:111","116:119") "s_nop 15" : A_OPS :: "memory");
    STAMP(4); CSTAMP(6);
    f32x16 o = oB;
    { const f32x16& g = oA;
      lds_st_f4((lds_ptr)lds + w * 3072 + lane * 16, (f32x4){g[0], g[1], g[2], g[3]}); lds_st_f4((lds_ptr)lds + w * 3072 + 1024 + lane * 16, (f32x4){g[4], g[5], g[6], g[7]});
      lds_st_f((lds_ptr)lds + w * 3072 + 2048 + lane * 4, g[8]);
      lds_bar();
      const f32x4 a0 = lds_f4((lds_cptr)lds + (w ^ 4) * 3072 + lane * 16), a1 = lds_f4((lds_cptr)lds + (w ^ 4) * 3072 + 1024 + lane * 16);
#pragma unroll
      for (int j = 0; j < 4; ++j) { o[j] += a0[j]; o[4 + j] += a1[j]; }
      o[8] += lds_f((lds_cptr)lds + (w ^ 4) * 3072 + 2048 + lane * 4); }
    float mx = fabsf(o[0]);
#pragma unroll
    for (int i = 1; i < 8; ++i) mx = fmaxf(mx, fabsf(o[i]));
    const bool ok = (o[8] < 1.0e18f) && (o[8] > 1.0e-18f) && (mx < 1.0e36f);
    if (__builtin_expect(!__all(ok), 0)) o = attn_exact(kv, qB, lane);
    const float inv = __uint_as_float(gu) * __builtin_amdgcn_rcpf(o[8]);
    gst16_dev(ws + WS_OF + ((tp * 2 + (w & 1)) * 32 + hg * 4 + ((w >> 1) & 1) * 2 + (w >> 2)) * 1024, lane * 16, pack8(o, 0, inv));
    wait_vm_bar<0>();
    if (w == HSW && lane < 8) __hip_atomic_store((gu64*)(ws + WS_OL + (tp * 8 + lane) * 128) + hg, tag, RLX_AGENT);
}

__global__ __launch_bounds__(512, 4) void k_all(const char* __restrict__ x, const char* __restrict__ wq, const char* __restrict__ wk, const char* __restrict__ wv, const char* __restrict__ wo,
                                                const char* __restrict__ wg, const char* __restrict__ wu, const char* __restrict__ wd, const char* __restrict__ gw,
                                                char* __restrict__ ws, char* __restrict__ out) {
    extern __shared__ __attribute__((aligned(16))) char lds[];
    const int tid = threadIdx.x, lane = tid & 63; const int w = RFL(tid >> 6);
    const int b = blockIdx.x;
    STAMP(0);
    const unsigned long long tag = (llvm_dispatch_id() + 1) * 0x9E3779B97F4A7C15ull + (unsigned long long)__builtin_amdgcn_dispatch_ptr();
    if ((b < 256) != (SWAP != 0)) {
        attn_role(b & 255, x, wq, ws, lds, w, lane, tag);
    } else {
        const int g = b & 255, xcd = g & 7, i = g >> 3;
        if (i >= 24) kv_job(8 * xcd + (i - 24), x, wk, wv, gw, ws, lds, w, lane, tag);
        if (i < 24) up_job<false>(4 * (xcd >> 1) + (i & 3), 7 * (xcd & 1) + (i >> 2), 0, x, wg, wu, ws, lds, w, lane, tag);
        else {
            up_job<true>(4 * (xcd >> 1) + (i & 3), 7 * (xcd & 1) + 6, (i >> 2) & 1, x, wg, wu, ws, lds, w, lane, tag);
        }
        STAMP(1);
        down_out_job(4 * xcd + (i >> 3), i & 7, wd, wo, ws, out, lds, w, lane, tag);
    }
    STAMP(7);
}

extern "C" void kernel_launch(void* const* d_in, const int* in_sizes, int n_in, void* d_out, int out_size, void* d_ws, size_t ws_size, hipStream_t stream) {
    (void)hipFuncSetAttribute((const void*)k_all, hipFuncAttributeMaxDynamicSharedMemorySize, 81920);
    k_all<<<512, 512, 81920, stream>>>((const char*)d_in[0], (const char*)d_in[1], (const char*)d_in[2], (const char*)d_in[3], (const char*)d_in[4],
                                       (const char*)d_in[6], (const char*)d_in[7], (const char*)d_in[8], (const char*)d_in[9], (char*)d_ws, (char*)d_out);
}
```

```cpp
#include <hip/hip_runtime.h>
#include <hip/hip_bf16.h>
#include <stdint.h>

typedef __attribute__((ext_vector_type(8))) short bf16x8;
typedef __attribute__((ext_vector_type(4))) short s16x4;
typedef __attribute__((ext_vector_type(16))) float f32x16;
typedef __attribute__((ext_vector_type(4))) float f32x4;
typedef __attribute__((ext_vector_type(4))) unsigned u32x4;
typedef __attribute__((ext_vector_type(2))) float f32x2;
typedef __attribute__((ext_vector_type(2))) __bf16 bf16x2_t;
typedef __attribute__((address_space(3))) const char* lds_cptr;
typedef __attribute__((address_space(3))) char* lds_ptr;
typedef __attribute__((address_space(1))) unsigned gu32;
typedef __attribute__((address_space(1))) unsigned long long gu64;
typedef short v4i16_t __attribute__((ext_vector_type(4)));
extern "C" __device__ unsigned long long llvm_dispatch_id() __asm("llvm.amdgcn.dispatch.id");
#define DI __device__ __forceinline__
#define MFMA(a, b, c) __builtin_amdgcn_mfma_f32_32x32x16_bf16((a), (b), (c), 0, 0, 0)
#define RLX_AGENT __ATOMIC_RELAXED, __HIP_MEMORY_SCOPE_AGENT
#define RFL(x) __builtin_amdgcn_readfirstlane(x)
#define SBAR() __builtin_amdgcn_sched_barrier(0)
#define PRIO_HI() __builtin_amdgcn_s_setprio(3)
#define PRIO_LO() __builtin_amdgcn_s_setprio(0)

constexpr int F_ = 1792;
constexpr float QSCALE = 0.25f * 1.4426950408889634f;
constexpr unsigned SPIN_CAP = 1u << 18;
constexpr int NREP = 8;

constexpr unsigned WS_G   = 4096;
constexpr unsigned WS_HL  = 16384;
constexpr unsigned WS_OL  = WS_HL + 32768;
constexpr unsigned WS_KV  = 131072;
constexpr unsigned WS_KVF = 262144;
constexpr unsigned WS_OF  = 327680;
constexpr unsigned WS_HF  = WS_OF + 2097152;

DI unsigned pack_bf16(float lo, float hi) { f32x2 v = {lo, hi}; bf16x2_t r = __builtin_convertvector(v, bf16x2_t); return __builtin_bit_cast(unsigned, r); }
DI void glds(const char* sbase, unsigned voff, unsigned lds_base) {
    unsigned sv;
    asm volatile("s_mov_b32 %0, m0\n\ts_mov_b32 m0, %3\n\ts_nop 0\n\tglobal_load_lds_dwordx4 %1, %2\n\ts_mov_b32 m0, %0"
                 : "=&s"(sv) : "v"(voff), "s"(sbase), "s"(RFL(lds_base)) : "memory");
}
DI void glds_dev(const char* sbase, unsigned voff, unsigned lds_base) {
    unsigned sv;
    asm volatile("s_mov_b32 %0, m0\n\ts_mov_b32 m0, %3\n\ts_nop 0\n\tglobal_load_lds_dwordx4 %1, %2 sc1\n\ts_mov_b32 m0, %0"
                 : "=&s"(sv) : "v"(voff), "s"(sbase), "s"(RFL(lds_base)) : "memory");
}
DI u32x4 gld16_async(const char* sbase, unsigned voff) { u32x4 r; asm volatile("global_load_dwordx4 %0, %1, %2" : "=v"(r) : "v"(voff), "s"(sbase) : "memory"); return r; }
DI u32x4 gld16_dev(const char* sbase, unsigned voff) { u32x4 r; asm volatile("global_load_dwordx4 %0, %1, %2 sc1" : "=v"(r) : "v"(voff), "s"(sbase) : "memory"); return r; }
DI void gst16_dev(char* sbase, unsigned voff, u32x4 v) { asm volatile("global_store_dwordx4 %0, %1, %2 sc1\n\ts_nop 1" :: "v"(voff), "v"(v), "s"(sbase) : "memory"); }
template <int N> DI void wait_vm_bar() { asm volatile("s_waitcnt vmcnt(%0) lgkmcnt(0)\n\ts_barrier" :: "i"(N) : "memory"); }
template <int N> DI void wait_vm_bar_lo() { asm volatile("s_setprio 0\n\ts_waitcnt vmcnt(%0) lgkmcnt(0)\n\ts_barrier\n\ts_setprio 3" :: "i"(N) : "memory"); }
DI void lds_bar_lo() { asm volatile("s_setprio 0\n\ts_waitcnt lgkmcnt(0)\n\ts_barrier\n\ts_setprio 3" ::: "memory"); }
DI void lds_bar() { asm volatile("s_waitcnt lgkmcnt(0)\n\ts_barrier" ::: "memory"); }
DI bf16x8 lds_b128(lds_cptr p) { return *(const __attribute__((address_space(3))) bf16x8*)p; }
DI f32x4 lds_f4(lds_cptr p) { return *(const __attribute__((address_space(3))) f32x4*)p; }
DI void lds_st_f4(lds_ptr p, f32x4 v) { *(__attribute__((address_space(3))) f32x4*)p = v; }
DI void lds_st_u4(lds_ptr p, u32x4 v) { *(__attribute__((address_space(3))) u32x4*)p = v; }
DI void lds_st_f(lds_ptr p, float v) { *(__attribute__((address_space(3))) float*)p = v; }
DI float lds_f(lds_cptr p) { return *(const __attribute__((address_space(3))) float*)p; }
DI s16x4 lds_tr(lds_cptr p) { return __builtin_bit_cast(s16x4, __builtin_amdgcn_ds_read_tr16_b64_v4i16((__attribute__((address_space(3))) v4i16_t*)p)); }
DI bf16x8 cat(s16x4 a, s16x4 b) { return __builtin_shufflevector(a, b, 0, 1, 2, 3, 4, 5, 6, 7); }
DI u32x4 pack8(const f32x16& x, int s, float sc) {
    u32x4 p;
    p[0] = pack_bf16(x[8 * s + 0] * sc, x[8 * s + 1] * sc); p[1] = pack_bf16(x[8 * s + 2] * sc, x[8 * s + 3] * sc);
    p[2] = pack_bf16(x[8 * s + 4] * sc, x[8 * s + 5] * sc); p[3] = pack_bf16(x[8 * s + 6] * sc, x[8 * s + 7] * sc);
    return p;
}
DI u32x4 pack44(f32x4 a, f32x4 b) { u32x4 p; p[0] = pack_bf16(a[0], a[1]); p[1] = pack_bf16(a[2], a[3]); p[2] = pack_bf16(b[0], b[1]); p[3] = pack_bf16(b[2], b[3]); return p; }

template <int SLP> DI void line_wait(const char* line, int n, unsigned long long tag, int lane) {
    const unsigned off = (lane < n ? lane : 0) * 8;
#pragma unroll 1
    for (unsigned s = 0; s < SPIN_CAP; ++s) {
        unsigned long long v;
        asm volatile("global_load_dwordx2 %0, %1, %2 sc1\n\ts_setprio 0\n\ts_waitcnt vmcnt(0)\n\ts_setprio 3" : "=v"(v) : "v"(off), "s"(line) : "memory");
        if (__all(v == tag)) break;
        PRIO_LO(); __builtin_amdgcn_s_sleep(SLP); PRIO_HI();
    }
}

DI void kv_job(int tile, const char* x, const char* wk, const char* wv, const char* gw, char* ws, char* lds, int w, int lane, unsigned long long tag) {
    PRIO_HI();
    constexpr int WB = 8192;
    const int h = lane >> 5, r32 = lane & 31;
    const unsigned sb = (unsigned)(uintptr_t)lds + w * WB;
    { const int row = lane >> 3; const unsigned xo = (tile * 32 + row) * 1024 + w * 128;
      const unsigned xe = xo + (((lane & 7) ^ (row >> 1)) * 16), xd = xo + (((lane & 7) ^ (4 + (row >> 1))) * 16);
      const unsigned wo_ = w * 2048 + lane * 16;
#pragma unroll
      for (int i = 0; i < 4; ++i) glds(x + i * 8192, (i & 1) ? xd : xe, sb + i * 1024);
      glds(wv, wo_, sb + 4096); glds(wv + 1024, wo_, sb + 5120); glds(wk, wo_, sb + 6144); glds(wk + 1024, wo_, sb + 7168);
      glds(gw, lane * 16, (unsigned)(uintptr_t)lds + 65536 + w * 1024); }
    asm volatile("s_waitcnt vmcnt(0)" ::: "memory");
    f32x16 a = {}, ga = {};
    const lds_cptr xb = (lds_cptr)lds + w * WB + r32 * 128;
    const lds_cptr wb = (lds_cptr)lds + w * WB + 4096 + ((lane >> 4) & 1) * 2048 + (8 * h + ((lane & 15) >> 2)) * 32 + (lane & 3) * 8;
    const lds_cptr gb = (lds_cptr)lds + 65536 + w * 1152 + h * 16;
#pragma unroll
    for (int ks = 0; ks < 4; ++ks) {
        const bf16x8 xf = lds_b128(xb + (((2 * ks + h) ^ ((r32 >> 1) & 7)) * 16));
        a = MFMA(cat(lds_tr(wb + ks * 512), lds_tr(wb + ks * 512 + 128)), xf, a); ga = MFMA(lds_b128(gb + ks * 32), xf, ga);
    }
    { lds_ptr p = (lds_ptr)lds + w * WB;
      lds_st_f4(p + lane * 16, (f32x4){a[8], a[9], a[10], a[11]}); lds_st_f4(p + 1024 + lane * 16, (f32x4){a[12], a[13], a[14], a[15]});
#pragma unroll
      for (int r = 0; r < 8; ++r) lds_st_f(p + 2048 + ((r & 3) + 8 * (r >> 2) + 4 * h) * 144 + r32 * 4, a[r]);
      lds_st_f(p + 4352 + lane * 4, ga[0]); }
    lds_bar();
    if (w < 2) {
        f32x4 s0 = {}, s1 = {};
        const lds_cptr p = (lds_cptr)lds + (w == 0 ? lane * 16 : 2048 + (lane & 15) * 144 + ((lane >> 4) & 1) * 16 + h * 64);
        const int d1 = w == 0 ? 1024 : 32;
#pragma unroll
        for (int q = 0; q < 8; ++q) { s0 += lds_f4(p + q * WB); s1 += lds_f4(p + q * WB + d1); }
        gst16_dev(ws + WS_KV + tile * 2048 + w * 1024, lane * 16, pack44(s0, s1));
    } else if (w == 2) {
        float s = 0.f;
#pragma unroll
        for (int q = 0; q < 8; ++q) s += lds_f((lds_cptr)lds + q * WB + 4352 + lane * 4);
        if (lane < 32) __hip_atomic_store((gu32*)(ws + WS_G) + tile * 32 + lane, __float_as_uint(1.0f / (1.0f + __expf(-s))), RLX_AGENT);
    }
    wait_vm_bar<0>();
    if (w == 0 && lane < NREP) __hip_atomic_store((gu64*)(ws + WS_KVF) + lane * 64 + tile, tag, RLX_AGENT);
    PRIO_LO();
}

template <bool HALF> DI void up_job(int rp, int cp, int half, const char* x, const char* wg, const char* wu, char* ws, char* lds, int w, int lane, unsigned long long tag) {
    constexpr int STB = 24576, NST = 16;
    const unsigned lds0 = (unsigned)(uintptr_t)lds + w * 1024;
    unsigned xs, gs;
    { int row = 16 * w + (lane >> 2); int c = (lane & 3) ^ ((row >> 2) & 3); xs = (rp * 128 + (HALF ? half * 64 + (row & 63) : row)) * 1024 + c * 16; }
    { int row = 4 * w + (lane >> 4); int c = (lane & 15) ^ ((row & 3) << 2); gs = row * (F_ * 2) + cp * 256 + c * 16; }
    int rot = 4 * (rp & 3);
#pragma unroll
    for (int s = 0; s < 2; ++s) { const unsigned b_ = lds0 + s * STB; const int k_ = (s + rot) & 15;
        glds(x + k_ * 64, xs, b_); glds(wg + k_ * (32 * F_ * 2), gs, b_ + 8192); glds(wu + k_ * (32 * F_ * 2), gs, b_ + 16384); }
    const int th = w & 1, fg = w >> 1, h = lane >> 5, r32 = lane & 31;
    lds_cptr xb[2], wb;
#pragma unroll
    for (int ks = 0; ks < 2; ++ks) { int tok = th * (HALF ? 32 : 64) + r32; xb[ks] = (lds_cptr)lds + tok * 64 + (((2 * ks + h) ^ ((tok >> 2) & 3)) * 16); }
    { const int g = (lane >> 4) & 1, q = (lane & 15) >> 2, p = lane & 3; const int col = fg * 32 + g * 16 + 4 * p;
      wb = (lds_cptr)lds + 8192 + (8 * h + q) * 256 + (((col >> 3) ^ (q << 2)) * 16) + (col & 7) * 2; }
    f32x16 ag[2] = {}, au[2] = {};
    unsigned g0, g1, goff = (rp * 128 + (HALF ? half * 64 + th * 32 : th * 64) + r32) * 4;
    unsigned foff = (lane & 3) * 8; unsigned long long gfl = 0;
    const char* gline = ws + WS_KVF + ((blockIdx.x & (NREP - 1)) * 64 + rp * 4) * 8;
    asm volatile("" : "+v"(goff), "+v"(foff), "+v"(gfl));
#define UP_RD(F, so, ks) do { F[0] = cat(lds_tr(wb + (so) + (ks) * 4096), lds_tr(wb + (so) + (ks) * 4096 + 1024)); F[1] = lds_b128(xb[ks] + (so)); if (!HALF) F[2] = lds_b128(xb[ks] + (so) + 2048); \
        F[3] = cat(lds_tr(wb + (so) + 8192 + (ks) * 4096), lds_tr(wb + (so) + 8192 + (ks) * 4096 + 1024)); } while (0)
    bf16x8 fa[4], fb[4];
#pragma unroll
    for (int s = 0; s < NST; ++s) {
        if (s + 1 < NST) wait_vm_bar<3>(); else wait_vm_bar<0>();
        const int so = (s % 3) * STB;
        SBAR(); UP_RD(fb, so, 0); SBAR();
        asm volatile("" : "+s"(rot));
        const unsigned b_ = lds0 + ((s + 2) % 3) * STB; const int k_ = (s + 2 + rot) & 15;
        if (s > 0) { ag[0] = MFMA(fa[0], fa[1], ag[0]); asm volatile("" : "+v"(ag[0])); }
        SBAR(); if (s + 2 < NST) glds(x + k_ * 64, xs, b_); SBAR();
        if (s > 0 && !HALF) { ag[1] = MFMA(fa[0], fa[2], ag[1]); asm volatile("" : "+v"(ag[1])); }
        SBAR(); if (s + 2 < NST) glds(wg + k_ * (32 * F_ * 2), gs, b_ + 8192); SBAR();
        if (s > 0) { au[0] = MFMA(fa[3], fa[1], au[0]); asm volatile("" : "+v"(au[0])); }
        SBAR(); if (s + 2 < NST) glds(wu + k_ * (32 * F_ * 2), gs, b_ + 16384); SBAR();
        if (s > 0 && !HALF) { au[1] = MFMA(fa[3], fa[2], au[1]); asm volatile("" : "+v"(au[1])); }
        SBAR();
        if (s == 8) asm volatile("s_cmp_lg_u32 %3, 0\n\ts_cbranch_scc1 1f\n\tglobal_load_dwordx2 %0, %1, %2 sc1\n1:" : "+v"(gfl) : "v"(foff), "s"(gline), "s"(w) : "memory", "scc");
        if (s == 11) { asm volatile("" : "+v"(gfl)); if (w == 0) { PRIO_HI(); if (!__all(gfl == tag)) line_wait<10>(gline, 4, tag, lane); PRIO_LO(); } }
        if (s == NST - 2) asm volatile("global_load_dword %0, %2, %3 sc1\n\tglobal_load_dword %1, %2, %3 offset:128 sc1" : "=&v"(g0), "=&v"(g1) : "v"(goff), "s"(ws + WS_G) : "memory");
        UP_RD(fa, so, 1); SBAR();
        ag[0] = MFMA(fb[0], fb[1], ag[0]); au[0] = MFMA(fb[3], fb[1], au[0]);
        if (!HALF) { ag[1] = MFMA(fb[0], fb[2], ag[1]); au[1] = MFMA(fb[3], fb[2], au[1]); }
        asm volatile("" : "+v"(ag[0]), "+v"(ag[1]), "+v"(au[0]), "+v"(au[1])); SBAR();
    }
    ag[0] = MFMA(fa[0], fa[1], ag[0]); au[0] = MFMA(fa[3], fa[1], au[0]);
    if (!HALF) { ag[1] = MFMA(fa[0], fa[2], ag[1]); au[1] = MFMA(fa[3], fa[2], au[1]); }
#undef UP_RD
    PRIO_HI();
    asm volatile("" : "+v"(g0), "+v"(g1));
#pragma unroll
    for (int i = 0; i < (HALF ? 1 : 2); ++i) {
        const float sc = 1.0f - __uint_as_float(i ? g1 : g0);
        f32x16 hh = ag[i] * au[i];
        char* dst = ws + WS_HF + ((rp * 4 + (HALF ? half * 2 + th : th * 2 + i)) * 112 + cp * 8 + fg * 2) * 1024;
        gst16_dev(dst, lane * 16, pack8(hh, 0, sc));
        gst16_dev(dst, 1024 + lane * 16, pack8(hh, 1, sc));
    }
    wait_vm_bar_lo<0>();
    if (w == 0 && lane < (HALF ? 8 : 16)) __hip_atomic_store((gu64*)(ws + WS_HL + (rp * 16 + (HALF ? half * 8 : 0) + lane) * 128) + cp, tag, RLX_AGENT);
}

DI void down_out_job(int tp, int cg, const char* wd, const char* wo, char* ws, char* out, char* lds, int w, int lane, unsigned long long tag) {
    constexpr int D = 4;
    const unsigned lds0 = (unsigned)(uintptr_t)lds;
    const int h = lane >> 5;
    const unsigned l16 = lane * 16;
    unsigned wsrc; { const int row = lane >> 3; wsrc = row * 1024 + cg * 128 + (((lane & 7) ^ (((row >> 1) & 1) << 2)) * 16); }
    int woff[2];
    { const int g = (lane >> 4) & 1, q = (lane & 15) >> 2, p = lane & 3, pp = ((p & 1) << 1) | (p >> 1), row = 4 * h + q;
#pragma unroll
      for (int ni = 0; ni < 2; ++ni) { const int col = ni * 32 + g * 16 + 4 * pp; woff[ni] = row * 128 + (((col >> 3) ^ (((row >> 1) & 1) << 2)) * 16) + (col & 7) * 2; } }
    const char* hx = ws + WS_HF + (tp * 224 + w) * 1024;
    const char* wdw = wd + w * 16384;
    const unsigned ring = RFL(lds0 + w * 10240);
    const lds_cptr ringp = (lds_cptr)lds + w * 10240;
    int rot = RFL((cg * 7 / 4 + (tp & 3) * 7 / 2) % 14);
    f32x16 acc[4] = {};
    u32x4 xr[D][2];
    const char* ol = ws + WS_OL + (tp * 8 + w) * 128;
    unsigned long long ofl; unsigned zero = 0; asm volatile("" : "+v"(zero));
    asm volatile("" : "+v"(acc[0]), "+v"(acc[1]), "+v"(acc[2]), "+v"(acc[3]));
    if (w == 0) line_wait<10>(ws + WS_HL + (tp * 8 + cg) * 128, 14, tag, lane);
    wait_vm_bar_lo<0>();
    PRIO_LO();
#define DN_ISSUE(i) do { \
        asm volatile("" : "+s"(rot)); \
        const int p_ = (i) + rot >= 14 ? (i) + rot - 14 : (i) + rot; \
        xr[(i) % D][0] = gld16_dev(hx + p_ * 8192, l16); xr[(i) % D][1] = gld16_dev(hx + p_ * 8192 + 114688, l16); \
        glds(wdw + p_ * 131072, wsrc, ring + ((i) % 5) * 2048); glds(wdw + p_ * 131072 + 8192, wsrc, ring + ((i) % 5) * 2048 + 1024); } while (0)
#define WO_ISSUE(j) do { glds(wo + (w * 4 + (j)) * 16384, wsrc, ring + (((j) + 14) % 5) * 2048); glds(wo + (w * 4 + (j)) * 16384 + 8192, wsrc, ring + (((j) + 14) % 5) * 2048 + 1024); } while (0)
#pragma unroll
    for (int i = 0; i < D; ++i) DN_ISSUE(i);
#pragma unroll
    for (int i = 0; i < 14; ++i) {
        const int full = (i + D - 1 < 14 ? D - 1 : 13 - i), half = D - 1 - full;
        asm volatile("s_waitcnt vmcnt(%2)" : "+v"(xr[i % D][0]), "+v"(xr[i % D][1]) : "i"(4 * full + 2 * half) : "memory");
        const lds_cptr rb = ringp + (i % 5) * 2048;
        const bf16x8 w0 = cat(lds_tr(rb + woff[0]), lds_tr(rb + woff[0] + 1024)), w1 = cat(lds_tr(rb + woff[1]), lds_tr(rb + woff[1] + 1024));
        const bf16x8 x0 = __builtin_bit_cast(bf16x8, xr[i % D][0]), x1 = __builtin_bit_cast(bf16x8, xr[i % D][1]);
        acc[0] = MFMA(w0, x0, acc[0]); acc[1] = MFMA(w0, x1, acc[1]); acc[2] = MFMA(w1, x0, acc[2]); acc[3] = MFMA(w1, x1, acc[3]);
        asm volatile("" : "+v"(acc[0]), "+v"(acc[1]), "+v"(acc[2]), "+v"(acc[3]));
        if (i + D < 14) DN_ISSUE(i + D); else WO_ISSUE(i + D - 14);
        if (i == 12) asm volatile("global_load_dwordx2 %0, %1, %2 sc1" : "=v"(ofl) : "v"(zero), "s"(ol) : "memory");
        if (i % 4 == 3) __builtin_amdgcn_s_barrier();
    }
#undef DN_ISSUE
#undef WO_ISSUE
    PRIO_HI();
    asm volatile("s_waitcnt vmcnt(2)" : "+v"(ofl) :: "memory");
    if (!__all(ofl == tag)) line_wait<6>(ol, 1, tag, lane);
    const char* ox = ws + WS_OF + (tp * 64 + w * 4) * 1024;
    u32x4 of[4][2];
#pragma unroll
    for (int i = 0; i < 4; ++i) { of[i][0] = gld16_dev(ox + i * 1024, l16); of[i][1] = gld16_dev(ox + i * 1024 + 32768, l16); }
#pragma unroll
    for (int i = 0; i < 4; ++i) {
        const lds_cptr rb = ringp + ((i + 14) % 5) * 2048;
        if (i == 0) asm volatile("s_waitcnt vmcnt(8)" ::: "memory");
        const bf16x8 w0 = cat(lds_tr(rb + woff[0]), lds_tr(rb + woff[0] + 1024)), w1 = cat(lds_tr(rb + woff[1]), lds_tr(rb + woff[1] + 1024));
        asm volatile("s_waitcnt vmcnt(%2)" : "+v"(of[i][0]), "+v"(of[i][1]) : "i"(6 - 2 * i) : "memory");
        const bf16x8 x0 = __builtin_bit_cast(bf16x8, of[i][0]), x1 = __builtin_bit_cast(bf16x8, of[i][1]);
        acc[0] = MFMA(w0, x0, acc[0]); acc[1] = MFMA(w0, x1, acc[1]); acc[2] = MFMA(w1, x0, acc[2]); acc[3] = MFMA(w1, x1, acc[3]);
    }
    f32x4 bq[2];
#pragma unroll
    for (int r = 0; r < 2; ++r) {
#pragma unroll
        for (int j = 0; j < 8; ++j) { const f32x16& a = acc[j & 3]; const int o = (j >> 2) * 8 + r * 4;
            lds_st_f4((lds_ptr)lds + (w * 10 + j) * 1024 + l16, (f32x4){a[o], a[o + 1], a[o + 2], a[o + 3]}); }
        lds_bar_lo();
        f32x4 sum = lds_f4((lds_cptr)lds + w * 1024 + l16);
#pragma unroll
        for (int q = 1; q < 8; ++q) sum += lds_f4((lds_cptr)lds + (q * 10 + w) * 1024 + l16);
        bq[r] = sum;
        if (r == 0) { asm volatile("" : "+v"(bq[0])); lds_bar(); }
    }
    const int tok = tp * 64 + (w & 1) * 32 + (lane & 31);
    *(u32x4*)(out + (size_t)tok * 1024 + (cg * 64 + ((w >> 1) & 1) * 32 + (w >> 2) * 16 + 8 * h) * 2) = pack44(bq[0], bq[1]);
}

constexpr int KVT = 2080;
DI float xhalf_max(float m) { auto rr = __builtin_amdgcn_permlane32_swap(__float_as_uint(m), __float_as_uint(m), false, false); return fmaxf(__uint_as_float(rr[0]), __uint_as_float(rr[1])); }
DI float max16(const f32x16& s) {
    float a = fmaxf(fmaxf(s[0], s[1]), s[2]), b = fmaxf(fmaxf(s[3], s[4]), s[5]);
    a = fmaxf(fmaxf(a, s[6]), s[7]); b = fmaxf(fmaxf(b, s[8]), s[9]); a = fmaxf(fmaxf(a, s[10]), s[11]); b = fmaxf(fmaxf(b, s[12]), s[13]);
    a = fmaxf(fmaxf(a, s[14]), s[15]); return fmaxf(a, b);
}
DI u32x4 exp_pack(const f32x16& s, int o) {
    u32x4 p;
#pragma unroll
    for (int i = 0; i < 4; ++i) p[i] = pack_bf16(__builtin_amdgcn_exp2f(s[o + 2 * i]), __builtin_amdgcn_exp2f(s[o + 2 * i + 1]));
    return p;
}
DI f32x16 attn_exact(const char* kv, bf16x8 qf, int lane) {
    f32x16 o = {}; float m = -INFINITY;
#pragma unroll 1
    for (int t = 0; t < 64; ++t) {
        u32x4 kf, v0, v1;
        asm volatile("global_load_dwordx4 %0, %3, %5 sc1\n\tglobal_load_dwordx4 %1, %4, %5 offset:1024 sc1\n\tglobal_load_dwordx4 %2, %4, %5 offset:1536 sc1\n\ts_waitcnt vmcnt(0)"
                     : "=&v"(kf), "=&v"(v0), "=&v"(v1) : "v"(t * 2048 + lane * 16), "v"(t * 2048 + (lane >> 5) * 256 + (lane & 15) * 16), "s"(kv) : "memory");
        if (lane & 16) { const unsigned c = (lane & 11) == 0 ? 0x3F803F80u : 0u; v0 = (u32x4){c, c, c, c}; v1 = v0; }
        f32x16 s = MFMA(__builtin_bit_cast(bf16x8, kf), qf, (f32x16){});
        const float mn = fmaxf(m, xhalf_max(max16(s)));
        const float al = __builtin_amdgcn_exp2f(m - mn);
        m = mn;
#pragma unroll
        for (int i = 0; i < 16; ++i) { o[i] *= al; s[i] -= mn; }
        o = MFMA(__builtin_bit_cast(bf16x8, v0), __builtin_bit_cast(bf16x8, exp_pack(s, 0)), o);
        o = MFMA(__builtin_bit_cast(bf16x8, v1), __builtin_bit_cast(bf16x8, exp_pack(s, 8)), o);
    }
    return o;
}
#define A_E(r) "v_exp_f32 v" #r ", v" #r "\n\t"
#define A_C(d, a, b) "v_cvt_pk_bf16_f32 v" #d ", v" #a ", v" #b "\n\t"
#define A_QK(d, q) "v_mfma_f32_32x32x16_bf16 v[" d "], v[100:103], v[" q "], v[80:95]\n\t"
#define A_PV(o, a, p) "v_mfma_f32_32x32x16_bf16 v[" o "], v[" a "], v[" p "], v[" o "]\n\t"
#define A_W(n) "s_waitcnt lgkmcnt(" #n ")\n\t"
#define A_RD(d, base, u) "ds_read_b128 v[" d "], " base " offset:((" u ")%%16)*4160\n\t"
#define A_U(S0,S1,S2,S3,S4,S5,S6,S7,S8,S9,S10,S11,S12,S13,S14,S15,P14,P15, M1, M2, M3) \
    A_E(S0) A_C(119,P14,P15) A_E(S1) A_E(S2) M1 A_E(S3) A_C(112,S0,S1) A_E(S4) A_E(S5) A_C(113,S2,S3) \
    M2 A_E(S6) A_E(S7) A_C(114,S4,S5) A_E(S8) A_E(S9) A_C(115,S6,S7) A_E(S10) A_E(S11) \
    M3 A_C(116,S8,S9) A_E(S12) A_E(S13) A_C(117,S10,S11) A_E(S14) A_E(S15) A_C(118,S12,S13)
#define A_TA(U) A_U(48,49,50,51,52,53,54,55,56,57,58,59,60,61,62,63,78,79, \
    A_PV("16:31","108:111","116:119") A_RD("108:111", "%11", U), A_QK("64:79","12:15") A_RD("100:103", "%9", U "+1"), A_W(2) A_PV("32:47","104:107","112:115"))
#define A_TB(U) A_U(64,65,66,67,68,69,70,71,72,73,74,75,76,77,78,79,62,63, \
    A_W(0) A_PV("32:47","108:111","116:119"), A_QK("48:63","96:99"), A_PV("16:31","104:107","112:115") A_RD("104:107", "%10", U "+1"))
#define A_OPS "+{v[32:47]}"(oA), "+{v[16:31]}"(oB), "+{v[48:63]}"(sA), "+{v[64:79]}"(sB), "+{v[100:103]}"(kn), "+{v[104:107]}"(v0), "+{v[108:111]}"(v1), "+{v[112:115]}"(p0), "+{v[116:119]}"(p1)
#define SP(n) "s_setprio " #n "\n\t"
#define A_CHUNK(C, BAR, G0, G1, P5, P7) \
    asm volatile(A_TA(#C "*4") A_TB(#C "*4") SP(0) A_TA(#C "*4+1") BAR SP(2) A_TB(#C "*4+1") G0 A_TA(#C "*4+2") G1 P5 A_TB(#C "*4+2") A_TA(#C "*4+3") P7 A_TB(#C "*4+3") \
                 : A_OPS : "v"(bk), "v"(bv0), "v"(bv1), "v"(kvo), "s"(kv + ((C) + 3) * 16384), "s"(dst0 + (((C) + 3) & 3) * 8 * KVT), "{v[80:95]}"(cn), "{v[96:99]}"(qA), "{v[12:15]}"(qB) : "memory")
#define A_BAR(n) "s_waitcnt vmcnt(" #n ")\n\ts_barrier\n\t"
#define A_G0 "s_mov_b32 m0, %14\n\ts_nop 0\n\tglobal_load_lds_dwordx4 %12, %13 sc1\n\t"
#define A_G1 "global_load_lds_dwordx4 %12, %13 offset:1024 sc1\n\t"
DI void attn_role(int blk, const char* x, const char* wq, char* ws, char* lds, int w, int lane, unsigned long long tag) {
    const int xcd = blk & 7;
    const int tp = (xcd >> 1) * 8 + ((blk >> 3) & 7), hg = (xcd & 1) * 4 + (blk >> 6);
    const unsigned lds0 = (unsigned)(uintptr_t)lds;
    const int kh = w >> 2;
    unsigned xs, wsr; int xoff[2], woff;
    { const int row = 8 * w + (lane >> 3);
      xs = (tp * 64 + row) * 1024 + (((lane & 7) ^ ((row >> 1) & 7)) * 16);
      wsr = row * 1024 + hg * 128 + (((lane & 7) ^ (((row >> 1) & 1) << 2)) * 16);
      const int h = lane >> 5, tok = (w & 1) * 32 + (lane & 31), col = ((w >> 1) & 1) * 32 + ((lane >> 4) & 1) * 16 + 4 * (lane & 3), q = (lane & 15) >> 2;
#pragma unroll
      for (int i = 0; i < 2; ++i) xoff[i] = tok * 128 + (((4 * kh + 2 * i + h) ^ ((tok >> 1) & 7)) * 16);
      woff = 8192 + (32 * kh + 8 * h + q) * 128 + (((col >> 3) ^ (((q >> 1) & 1) << 2)) * 16) + (col & 7) * 2; }
    const int qrot = 2 * (hg & 3) + tp;
    if (blk >= 192) __builtin_amdgcn_s_sleep(40);
#pragma unroll
    for (int s = 0; s < 5; ++s) { const unsigned b_ = lds0 + w * 1024 + ((s + 4) % 5) * 16384; const int k_ = (s + qrot) & 7; glds(x + k_ * 128, xs, b_); glds(wq + k_ * 65536, wsr, b_ + 8192); }
    u32x4 qr[3][2];
#pragma unroll
    for (int j = 0; j < 3; ++j) { const int k_ = (5 + j + qrot) & 7; qr[j][0] = gld16_async(x + k_ * 128, xs); qr[j][1] = gld16_async(wq + k_ * 65536, wsr); }
    f32x16 acc = {};
    bf16x8 fx[8], fw[8];
#define Q_RD(s, o) do { const lds_cptr b_ = (lds_cptr)lds + (((s) + 4) % 5) * 16384; _Pragma("unroll") for (int i_ = 0; i_ < 2; ++i_) { \
        fw[(o) + i_] = cat(lds_tr(b_ + woff + i_ * 2048), lds_tr(b_ + woff + i_ * 2048 + 512)); fx[(o) + i_] = lds_b128(b_ + xoff[i_]); } } while (0)
#define Q_MM(a_, b_) do { _Pragma("unroll") for (int i_ = (a_); i_ < (b_); ++i_) acc = MFMA(fw[i_], fx[i_], acc); asm volatile("" : "+v"(acc)); } while (0)
    wait_vm_bar<6>();
    SBAR(); Q_RD(0, 0); Q_RD(1, 2); Q_RD(2, 4); Q_RD(3, 6); SBAR();
    Q_MM(0, 2); SBAR(); Q_RD(4, 0); SBAR(); Q_MM(2, 4); SBAR();
    asm volatile("s_waitcnt vmcnt(0) lgkmcnt(0)\n\ts_barrier" : "+v"(qr[0][0]), "+v"(qr[0][1]), "+v"(qr[1][0]), "+v"(qr[1][1]), "+v"(qr[2][0]), "+v"(qr[2][1]) :: "memory");
#pragma unroll
    for (int j = 0; j < 3; ++j) { lds_ptr p_ = (lds_ptr)lds + w * 1024 + lane * 16 + ((j + 9) % 5) * 16384; lds_st_u4(p_, qr[j][0]); lds_st_u4(p_ + 8192, qr[j][1]); }
    SBAR();
    Q_MM(4, 8); SBAR();
    lds_bar();
    SBAR(); Q_RD(5, 2); Q_RD(6, 4); Q_RD(7, 6); SBAR();
    Q_MM(0, 8); SBAR();
#undef Q_RD
#undef Q_MM
#pragma unroll
    for (int i = 0; i < 4; ++i) lds_st_f4((lds_ptr)lds + 32768 + w * 4096 + i * 1024 + lane * 16, (f32x4){acc[4 * i], acc[4 * i + 1], acc[4 * i + 2], acc[4 * i + 3]});
    lds_bar();
#pragma unroll
    for (int i = 0; i < 4; ++i) { const f32x4 v = lds_f4((lds_cptr)lds + 32768 + (w ^ 4) * 4096 + i * 1024 + lane * 16);
#pragma unroll
        for (int j = 0; j < 4; ++j) acc[4 * i + j] += v[j]; }
    const u32x4 q0 = pack8(acc, 0, QSCALE), q1 = pack8(acc, 1, QSCALE);
    bf16x8 qA = __builtin_bit_cast(bf16x8, kh ? q0 : q1), qB = __builtin_bit_cast(bf16x8, kh ? q1 : q0);
    const char* kv = ws + WS_KV;
    unsigned kvo = w * 2048 + lane * 16, goff = (tp * 64 + (w & 1) * 32 + (lane & 31)) * 4;
    const unsigned dst0 = RFL(lds0 + w * KVT);
    unsigned bk = lds0 + kh * KVT + lane * 16;
    const unsigned pad = lds0 + 2048 + ((lane & 11) == 0 ? 0 : 16);
    unsigned bv0 = (lane & 16) ? pad : lds0 + kh * KVT + 1024 + (lane >> 5) * 256 + (lane & 15) * 16;
    unsigned bv1 = (lane & 16) ? pad : bv0 + 512;
    f32x16 oA = {}, oB = {}, sB = {};
    u32x4 p0 = {0u, 0u, 0u, 0u}, p1 = {0u, 0u, 0u, 0u};
    bf16x8 v1 = {};
    asm volatile("" : "+{v[96:99]}"(qA), "+{v[12:15]}"(qB), "+v"(kvo), "+v"(goff), "+v"(bk), "+v"(bv0), "+v"(bv1), "+{v[32:47]}"(oA), "+{v[16:31]}"(oB), "+{v[112:115]}"(p0), "+{v[116:119]}"(p1), "+{v[108:111]}"(v1));
    if (w == 0) line_wait<15>(ws + WS_KVF + (blk & (NREP - 1)) * 512, 64, tag, lane);
    PRIO_LO();
    lds_bar();
#pragma unroll
    for (int c = 0; c < 4; ++c) { glds_dev(kv + c * 16384, kvo, dst0 + c * 8 * KVT); glds_dev(kv + c * 16384 + 1024, kvo, dst0 + c * 8 * KVT + 1024); }
    unsigned gu; asm volatile("global_load_dword %0, %1, %2 sc1" : "=v"(gu) : "v"(goff), "s"(ws + WS_G) : "memory");
    if (w == 1) { const unsigned c = (lane & 1) ? 0u : 0x3F803F80u; lds_st_u4((lds_ptr)lds + (lane >> 1) * KVT + 2048 + (lane & 1) * 16, (u32x4){c, c, c, c}); }
    wait_vm_bar<7>();
    f32x16 cn, sA;
    bf16x8 kn = lds_b128((lds_cptr)(uintptr_t)bk);
    bf16x8 v0 = lds_b128((lds_cptr)(uintptr_t)bv0);
    { const bf16x8 k0 = lds_b128((lds_cptr)lds + lane * 16);
      sA = MFMA(k0, qA, (f32x16){}); sB = MFMA(k0, qB, (f32x16){});
      const float m = xhalf_max(fmaxf(max16(sA), max16(sB)));
#pragma unroll
      for (int i = 0; i < 16; ++i) cn[i] = -m; }
    sA = MFMA(kn, qA, cn); sB[14] = 0.f; sB[15] = 0.f;
    unsigned m0save; asm volatile("s_mov_b32 %0, m0\n\ts_nop 15" : "=s"(m0save), A_OPS);
    A_CHUNK(0, A_BAR(5), "", "", "", SP(1));
    A_CHUNK(1, A_BAR(3), A_G0, A_G1, "", SP(1));
    A_CHUNK(2, A_BAR(2), A_G0, A_G1, "", SP(1));
    A_CHUNK(3, A_BAR(2), A_G0, A_G1, "", SP(1));
    A_CHUNK(4, A_BAR(2), A_G0, A_G1, "", SP(1));
    A_CHUNK(5, A_BAR(2), "", "", "", SP(1));
    A_CHUNK(6, A_BAR(0), "", "", "", SP(1));
    A_CHUNK(7, "s_barrier\n\t", "", "", SP(1), SP(0));
    asm volatile("s_mov_b32 m0, %1" : "+v"(gu) : "s"(m0save));
    asm volatile("s_setprio 3\n\t" A_C(119,78,79) "s_waitcnt lgkmcnt(0)\n\ts_nop 1\n\t" A_PV("16:31","108:111","116:119") "s_nop 15" : A_OPS :: "memory");
    int ln; asm volatile("v_mbcnt_lo_u32_b32 %0, -1, 0\n\tv_mbcnt_hi_u32_b32 %0, -1, %0" : "=v"(ln));
    f32x16 o = oB;
    { lds_st_f4((lds_ptr)lds + w * 3072 + ln * 16, (f32x4){oA[0], oA[1], oA[2], oA[3]}); lds_st_f4((lds_ptr)lds + w * 3072 + 1024 + ln * 16, (f32x4){oA[4], oA[5], oA[6], oA[7]});
      lds_st_f((lds_ptr)lds + w * 3072 + 2048 + ln * 4, oA[8]);
      lds_bar();
      const f32x4 a0 = lds_f4((lds_cptr)lds + (w ^ 4) * 3072 + ln * 16), a1 = lds_f4((lds_cptr)lds + (w ^ 4) * 3072 + 1024 + ln * 16);
#pragma unroll
      for (int j = 0; j < 4; ++j) { o[j] += a0[j]; o[4 + j] += a1[j]; }
      o[8] += lds_f((lds_cptr)lds + (w ^ 4) * 3072 + 2048 + ln * 4); }
    float mx = fabsf(o[0]);
#pragma unroll
    for (int i = 1; i < 8; ++i) mx = fmaxf(mx, fabsf(o[i]));
    const bool ok = (o[8] < 1.0e18f) && (o[8] > 1.0e-18f) && (mx < 1.0e36f);
    if (__builtin_expect(!__all(ok), 0)) o = attn_exact(kv, qB, ln);
    const float inv = __uint_as_float(gu) * __builtin_amdgcn_rcpf(o[8]);
    gst16_dev(ws + WS_OF + ((tp * 2 + (w & 1)) * 32 + hg * 4 + ((w >> 1) & 1) * 2 + (w >> 2)) * 1024, ln * 16, pack8(o, 0, inv));
    wait_vm_bar<0>();
    if (w == 0 && ln == 0) __hip_atomic_store((gu64*)(ws + WS_OL + (tp * 8 + hg) * 128), tag, RLX_AGENT);
}

__global__ __launch_bounds__(512, 4) void k_all(const char* __restrict__ x, const char* __restrict__ wq, const char* __restrict__ wk, const char* __restrict__ wv, const char* __restrict__ wo,
                                                const char* __restrict__ wg, const char* __restrict__ wu, const char* __restrict__ wd, const char* __restrict__ gw,
                                                char* __restrict__ ws, char* __restrict__ out) {
    extern __shared__ __attribute__((aligned(16))) char lds[];
    const int tid = threadIdx.x, lane = tid & 63; const int w = RFL(tid >> 6);
    const int b = blockIdx.x;
    const unsigned long long tag = (llvm_dispatch_id() + 1) * 0x9E3779B97F4A7C15ull + (unsigned long long)__builtin_amdgcn_dispatch_ptr();
    if (b < 256) {
        attn_role(b, x, wq, ws, lds, w, lane, tag);
    } else {
        const int g = b & 255, xcd = g & 7, i = g >> 3;
        if (i >= 24) kv_job(8 * xcd + (i - 24), x, wk, wv, gw, ws, lds, w, lane, tag);
        if (i < 24) up_job<false>(4 * (xcd >> 1) + (i & 3), 7 * (xcd & 1) + (i >> 2), 0, x, wg, wu, ws, lds, w, lane, tag);
        else up_job<true>(4 * (xcd >> 1) + (i & 3), 7 * (xcd & 1) + 6, (i >> 2) & 1, x, wg, wu, ws, lds, w, lane, tag);
        down_out_job(4 * xcd + (i >> 3), i & 7, wd, wo, ws, out, lds, w, lane, tag);
    }
}

extern "C" void kernel_launch(void* const* d_in, const int* in_sizes, int n_in, void* d_out, int out_size, void* d_ws, size_t ws_size, hipStream_t stream) {
    (void)hipFuncSetAttribute((const void*)k_all, hipFuncAttributeMaxDynamicSharedMemorySize, 81920);
    k_all<<<512, 512, 81920, stream>>>((const char*)d_in[0], (const char*)d_in[1], (const char*)d_in[2], (const char*)d_in[3], (const char*)d_in[4],
                                       (const char*)d_in[6], (const char*)d_in[7], (const char*)d_in[8], (const char*)d_in[9], (char*)d_ws, (char*)d_out);
}
```

```cpp
#include <hip/hip_runtime.h>
#include <hip/hip_bf16.h>
#include <stdint.h>

typedef __attribute__((ext_vector_type(8))) short bf16x8;
typedef __attribute__((ext_vector_type(4))) short s16x4;
typedef __attribute__((ext_vector_type(16))) float f32x16;
typedef __attribute__((ext_vector_type(4))) float f32x4;
typedef __attribute__((ext_vector_type(4))) unsigned u32x4;
typedef __attribute__((ext_vector_type(2))) float f32x2;
typedef __attribute__((ext_vector_type(2))) __bf16 bf16x2_t;
typedef __attribute__((address_space(3))) const char* lds_cptr;
typedef __attribute__((address_space(3))) char* lds_ptr;
typedef __attribute__((address_space(1))) unsigned gu32;
typedef __attribute__((address_space(1))) unsigned long long gu64;
typedef short v4i16_t __attribute__((ext_vector_type(4)));
extern "C" __device__ unsigned long long llvm_dispatch_id() __asm("llvm.amdgcn.dispatch.id");
#define DI __device__ __forceinline__
#define MFMA(a, b, c) __builtin_amdgcn_mfma_f32_32x32x16_bf16((a), (b), (c), 0, 0, 0)
#define RLX_AGENT __ATOMIC_RELAXED, __HIP_MEMORY_SCOPE_AGENT
#define RFL(x) __builtin_amdgcn_readfirstlane(x)
#define SBAR() __builtin_amdgcn_sched_barrier(0)
#define PRIO_HI() __builtin_amdgcn_s_setprio(3)
#define PRIO_LO() __builtin_amdgcn_s_setprio(0)

constexpr int F_ = 1792;
constexpr float QSCALE = 0.25f * 1.4426950408889634f;
constexpr unsigned SPIN_CAP = 1u << 18;
constexpr int NREP = 8;

constexpr unsigned WS_G   = 4096;
constexpr unsigned WS_HL  = 16384;
constexpr unsigned WS_OL  = WS_HL + 32768;
constexpr unsigned WS_KV  = 131072;
constexpr unsigned WS_KVF = 262144;
constexpr unsigned WS_OF  = 327680;
constexpr unsigned WS_HF  = WS_OF + 2097152;

DI unsigned pack_bf16(float lo, float hi) { f32x2 v = {lo, hi}; bf16x2_t r = __builtin_convertvector(v, bf16x2_t); return __builtin_bit_cast(unsigned, r); }
DI void glds(const char* sbase, unsigned voff, unsigned lds_base) {
    unsigned sv;
    asm volatile("s_mov_b32 %0, m0\n\ts_mov_b32 m0, %3\n\ts_nop 0\n\tglobal_load_lds_dwordx4 %1, %2\n\ts_mov_b32 m0, %0"
                 : "=&s"(sv) : "v"(voff), "s"(sbase), "s"(RFL(lds_base)) : "memory");
}
DI void glds_dev(const char* sbase, unsigned voff, unsigned lds_base) {
    unsigned sv;
    asm volatile("s_mov_b32 %0, m0\n\ts_mov_b32 m0, %3\n\ts_nop 0\n\tglobal_load_lds_dwordx4 %1, %2 sc1\n\ts_mov_b32 m0, %0"
                 : "=&s"(sv) : "v"(voff), "s"(sbase), "s"(RFL(lds_base)) : "memory");
}
DI u32x4 gld16_async(const char* sbase, unsigned voff) { u32x4 r; asm volatile("global_load_dwordx4 %0, %1, %2" : "=v"(r) : "v"(voff), "s"(sbase) : "memory"); return r; }
DI u32x4 gld16_dev(const char* sbase, unsigned voff) { u32x4 r; asm volatile("global_load_dwordx4 %0, %1, %2 sc1" : "=v"(r) : "v"(voff), "s"(sbase) : "memory"); return r; }
DI void gst16_dev(char* sbase, unsigned voff, u32x4 v) { asm volatile("global_store_dwordx4 %0, %1, %2 sc1\n\ts_nop 1" :: "v"(voff), "v"(v), "s"(sbase) : "memory"); }
template <int N> DI void wait_vm_bar() { asm volatile("s_waitcnt vmcnt(%0) lgkmcnt(0)\n\ts_barrier" :: "i"(N) : "memory"); }
template <int N> DI void wait_vm_bar_lo() { asm volatile("s_setprio 0\n\ts_waitcnt vmcnt(%0) lgkmcnt(0)\n\ts_barrier\n\ts_setprio 3" :: "i"(N) : "memory"); }
DI void lds_bar_lo() { asm volatile("s_setprio 0\n\ts_waitcnt lgkmcnt(0)\n\ts_barrier\n\ts_setprio 3" ::: "memory"); }
DI void lds_bar() { asm volatile("s_waitcnt lgkmcnt(0)\n\ts_barrier" ::: "memory"); }
DI bf16x8 lds_b128(lds_cptr p) { return *(const __attribute__((address_space(3))) bf16x8*)p; }
DI f32x4 lds_f4(lds_cptr p) { return *(const __attribute__((address_space(3))) f32x4*)p; }
DI void lds_st_f4(lds_ptr p, f32x4 v) { *(__attribute__((address_space(3))) f32x4*)p = v; }
DI void lds_st_u4(lds_ptr p, u32x4 v) { *(__attribute__((address_space(3))) u32x4*)p = v; }
DI void lds_st_f(lds_ptr p, float v) { *(__attribute__((address_space(3))) float*)p = v; }
DI float lds_f(lds_cptr p) { return *(const __attribute__((address_space(3))) float*)p; }
DI s16x4 lds_tr(lds_cptr p) { return __builtin_bit_cast(s16x4, __builtin_amdgcn_ds_read_tr16_b64_v4i16((__attribute__((address_space(3))) v4i16_t*)p)); }
DI bf16x8 cat(s16x4 a, s16x4 b) { return __builtin_shufflevector(a, b, 0, 1, 2, 3, 4, 5, 6, 7); }
DI u32x4 pack8(const f32x16& x, int s, float sc) {
    u32x4 p;
    p[0] = pack_bf16(x[8 * s + 0] * sc, x[8 * s + 1] * sc); p[1] = pack_bf16(x[8 * s + 2] * sc, x[8 * s + 3] * sc);
    p[2] = pack_bf16(x[8 * s + 4] * sc, x[8 * s + 5] * sc); p[3] = pack_bf16(x[8 * s + 6] * sc, x[8 * s + 7] * sc);
    return p;
}
DI u32x4 pack44(f32x4 a, f32x4 b) { u32x4 p; p[0] = pack_bf16(a[0], a[1]); p[1] = pack_bf16(a[2], a[3]); p[2] = pack_bf16(b[0], b[1]); p[3] = pack_bf16(b[2], b[3]); return p; }

template <int SLP> DI void line_wait(const char* line, int n, unsigned long long tag, int lane) {
    const unsigned off = (lane < n ? lane : 0) * 8;
#pragma unroll 1
    for (unsigned s = 0; s < SPIN_CAP; ++s) {
        unsigned long long v;
        asm volatile("global_load_dwordx2 %0, %1, %2 sc1\n\ts_setprio 0\n\ts_waitcnt vmcnt(0)\n\ts_setprio 3" : "=v"(v) : "v"(off), "s"(line) : "memory");
        if (__all(v == tag)) break;
        PRIO_LO(); __builtin_amdgcn_s_sleep(SLP); PRIO_HI();
    }
}

DI void kv_job(int tile, const char* x, const char* wk, const char* wv, const char* gw, char* ws, char* lds, int w, int lane, unsigned long long tag) {
    PRIO_HI();
    constexpr int WB = 8192;
    const int h = lane >> 5, r32 = lane & 31;
    const unsigned sb = (unsigned)(uintptr_t)lds + w * WB;
    { const int row = lane >> 3; const unsigned xo = (tile * 32 + row) * 1024 + w * 128;
      const unsigned xe = xo + (((lane & 7) ^ (row >> 1)) * 16), xd = xo + (((lane & 7) ^ (4 + (row >> 1))) * 16);
      const unsigned wo_ = w * 2048 + lane * 16;
#pragma unroll
      for (int i = 0; i < 4; ++i) glds(x + i * 8192, (i & 1) ? xd : xe, sb + i * 1024);
      glds(wv, wo_, sb + 4096); glds(wv + 1024, wo_, sb + 5120); glds(wk, wo_, sb + 6144); glds(wk + 1024, wo_, sb + 7168);
      glds(gw, lane * 16, (unsigned)(uintptr_t)lds + 65536 + w * 1024); }
    f32x16 a = {}, ga = {};
    const lds_cptr xb = (lds_cptr)lds + w * WB + r32 * 128;
    const lds_cptr wb = (lds_cptr)lds + w * WB + 4096 + ((lane >> 4) & 1) * 2048 + (8 * h + ((lane & 15) >> 2)) * 32 + (lane & 3) * 8;
    const lds_cptr gb = (lds_cptr)lds + 65536 + w * 1152 + h * 16;
    lds_cptr rp0 = (lds_cptr)lds + (w == 0 ? lane * 16 : 2048 + (lane & 15) * 144 + ((lane >> 4) & 1) * 16 + h * 64), rp1 = rp0 + (w == 0 ? 1024 : 32);
    asm volatile("" : "+v"(rp0), "+v"(rp1));
    SBAR();
    asm volatile("s_waitcnt vmcnt(0)" ::: "memory");
    bf16x8 xf[4], wf[4], gf[4];
#pragma unroll
    for (int ks = 0; ks < 4; ++ks) { xf[ks] = lds_b128(xb + (((2 * ks + h) ^ ((r32 >> 1) & 7)) * 16)); wf[ks] = cat(lds_tr(wb + ks * 512), lds_tr(wb + ks * 512 + 128)); }
#pragma unroll
    for (int ks = 0; ks < 4; ++ks) gf[ks] = lds_b128(gb + ks * 32);
    SBAR();
#pragma unroll
    for (int ks = 0; ks < 4; ++ks) a = MFMA(wf[ks], xf[ks], a);
#pragma unroll
    for (int ks = 0; ks < 4; ++ks) ga = MFMA(gf[ks], xf[ks], ga);
    { lds_ptr p = (lds_ptr)lds + w * WB;
      lds_st_f4(p + lane * 16, (f32x4){a[8], a[9], a[10], a[11]}); lds_st_f4(p + 1024 + lane * 16, (f32x4){a[12], a[13], a[14], a[15]});
#pragma unroll
      for (int r = 0; r < 8; ++r) lds_st_f(p + 2048 + ((r & 3) + 8 * (r >> 2) + 4 * h) * 144 + r32 * 4, a[r]);
      lds_st_f(p + 4352 + lane * 4, ga[0]); }
    lds_bar();
    if (w < 2) {
        f32x4 r0[8], r1[8];
#pragma unroll
        for (int q = 0; q < 8; ++q) { r0[q] = lds_f4(rp0 + q * WB); r1[q] = lds_f4(rp1 + q * WB); }
        SBAR();
        const f32x4 s0 = ((r0[0] + r0[1]) + (r0[2] + r0[3])) + ((r0[4] + r0[5]) + (r0[6] + r0[7])), s1 = ((r1[0] + r1[1]) + (r1[2] + r1[3])) + ((r1[4] + r1[5]) + (r1[6] + r1[7]));
        gst16_dev(ws + WS_KV + tile * 2048 + w * 1024, lane * 16, pack44(s0, s1));
    } else if (w == 2) {
        float s = 0.f;
#pragma unroll
        for (int q = 0; q < 8; ++q) s += lds_f((lds_cptr)lds + q * WB + 4352 + lane * 4);
        if (lane < 32) __hip_atomic_store((gu32*)(ws + WS_G) + tile * 32 + lane, __float_as_uint(__builtin_amdgcn_rcpf(1.0f + __builtin_amdgcn_exp2f(-1.4426950408889634f * s))), RLX_AGENT);
    }
    wait_vm_bar<0>();
    if (w == 0 && lane < NREP) __hip_atomic_store((gu64*)(ws + WS_KVF) + lane * 64 + tile, tag, RLX_AGENT);
    PRIO_LO();
}

template <bool HALF> DI void up_job(int rp, int cp, int half, const char* x, const char* wg, const char* wu, char* ws, char* lds, int w, int lane, unsigned long long tag) {
    constexpr int STB = 24576, NST = 16;
    const unsigned lds0 = (unsigned)(uintptr_t)lds + w * 1024;
    unsigned xs, gs;
    { int row = 16 * w + (lane >> 2); int c = (lane & 3) ^ ((row >> 2) & 3); xs = (rp * 128 + (HALF ? half * 64 + (row & 63) : row)) * 1024 + c * 16; }
    { int row = 4 * w + (lane >> 4); int c = (lane & 15) ^ ((row & 3) << 2); gs = row * (F_ * 2) + cp * 256 + c * 16; }
    int rot = 4 * (rp & 3);
#pragma unroll
    for (int s = 0; s < 2; ++s) { const unsigned b_ = lds0 + s * STB; const int k_ = (s + rot) & 15;
        glds(x + k_ * 64, xs, b_); glds(wg + k_ * (32 * F_ * 2), gs, b_ + 8192); glds(wu + k_ * (32 * F_ * 2), gs, b_ + 16384); }
    const int th = w & 1, fg = w >> 1, h = lane >> 5, r32 = lane & 31;
    lds_cptr xb[2], wb;
#pragma unroll
    for (int ks = 0; ks < 2; ++ks) { int tok = th * (HALF ? 32 : 64) + r32; xb[ks] = (lds_cptr)lds + tok * 64 + (((2 * ks + h) ^ ((tok >> 2) & 3)) * 16); }
    { const int g = (lane >> 4) & 1, q = (lane & 15) >> 2, p = lane & 3; const int col = fg * 32 + g * 16 + 4 * p;
      wb = (lds_cptr)lds + 8192 + (8 * h + q) * 256 + (((col >> 3) ^ (q << 2)) * 16) + (col & 7) * 2; }
    f32x16 ag[2] = {}, au[2] = {};
    unsigned g0, g1, goff = (rp * 128 + (HALF ? half * 64 + th * 32 : th * 64) + r32) * 4;
    unsigned foff = (lane & 3) * 8; unsigned long long gfl = 0;
    const char* gline = ws + WS_KVF + ((blockIdx.x & (NREP - 1)) * 64 + rp * 4) * 8;
    lds_cptr wb2 = wb + 8192;
    asm volatile("" : "+v"(goff), "+v"(foff), "+v"(gfl), "+v"(xb[0]), "+v"(xb[1]), "+v"(wb), "+v"(wb2));
#define UP_RD(F, so, ks) do { F[0] = cat(lds_tr(wb + (so) + (ks) * 4096), lds_tr(wb + (so) + (ks) * 4096 + 1024)); F[1] = lds_b128(xb[ks] + (so)); if (!HALF) F[2] = lds_b128(xb[ks] + (so) + 2048); \
        F[3] = cat(lds_tr(wb2 + (so) + (ks) * 4096), lds_tr(wb2 + (so) + (ks) * 4096 + 1024)); } while (0)
    bf16x8 fa[4], fb[4];
#pragma unroll
    for (int s = 0; s < NST; ++s) {
        if (s + 1 < NST) wait_vm_bar<3>(); else wait_vm_bar<0>();
        const int so = (s % 3) * STB;
        SBAR(); UP_RD(fb, so, 0); SBAR();
        asm volatile("" : "+s"(rot));
        const unsigned b_ = lds0 + ((s + 2) % 3) * STB; const int k_ = (s + 2 + rot) & 15;
        if (s > 0) { ag[0] = MFMA(fa[0], fa[1], ag[0]); asm volatile("" : "+v"(ag[0])); }
        SBAR(); if (s + 2 < NST) glds(x + k_ * 64, xs, b_); SBAR();
        if (s > 0 && !HALF) { ag[1] = MFMA(fa[0], fa[2], ag[1]); asm volatile("" : "+v"(ag[1])); }
        SBAR(); if (s + 2 < NST) glds(wg + k_ * (32 * F_ * 2), gs, b_ + 8192); SBAR();
        if (s > 0) { au[0] = MFMA(fa[3], fa[1], au[0]); asm volatile("" : "+v"(au[0])); }
        SBAR(); if (s + 2 < NST) glds(wu + k_ * (32 * F_ * 2), gs, b_ + 16384); SBAR();
        if (s > 0 && !HALF) { au[1] = MFMA(fa[3], fa[2], au[1]); asm volatile("" : "+v"(au[1])); }
        SBAR();
        if (s == 8) asm volatile("s_cmp_lg_u32 %3, 0\n\ts_cbranch_scc1 1f\n\tglobal_load_dwordx2 %0, %1, %2 sc1\n1:" : "+v"(gfl) : "v"(foff), "s"(gline), "s"(w) : "memory", "scc");
        if (s == 11) { asm volatile("" : "+v"(gfl)); if (w == 0) { PRIO_HI(); if (!__all(gfl == tag)) line_wait<10>(gline, 4, tag, lane); PRIO_LO(); } }
        if (s == NST - 2) asm volatile("global_load_dword %0, %2, %3 sc1\n\tglobal_load_dword %1, %2, %3 offset:128 sc1" : "=&v"(g0), "=&v"(g1) : "v"(goff), "s"(ws + WS_G) : "memory");
        UP_RD(fa, so, 1); SBAR();
        ag[0] = MFMA(fb[0], fb[1], ag[0]); au[0] = MFMA(fb[3], fb[1], au[0]);
        if (!HALF) { ag[1] = MFMA(fb[0], fb[2], ag[1]); au[1] = MFMA(fb[3], fb[2], au[1]); }
        asm volatile("" : "+v"(ag[0]), "+v"(au[0]));
        if (!HALF) asm volatile("" : "+v"(ag[1]), "+v"(au[1]));
        SBAR();
    }
    ag[0] = MFMA(fa[0], fa[1], ag[0]); au[0] = MFMA(fa[3], fa[1], au[0]);
    if (!HALF) { ag[1] = MFMA(fa[0], fa[2], ag[1]); au[1] = MFMA(fa[3], fa[2], au[1]); }
#undef UP_RD
    PRIO_HI();
    asm volatile("" : "+v"(g0), "+v"(g1));
#pragma unroll
    for (int i = 0; i < (HALF ? 1 : 2); ++i) {
        const float sc = 1.0f - __uint_as_float(i ? g1 : g0);
        f32x16 hh = ag[i] * au[i];
        char* dst = ws + WS_HF + ((rp * 4 + (HALF ? half * 2 + th : th * 2 + i)) * 112 + cp * 8 + fg * 2) * 1024;
        gst16_dev(dst, lane * 16, pack8(hh, 0, sc));
        gst16_dev(dst, 1024 + lane * 16, pack8(hh, 1, sc));
    }
    wait_vm_bar_lo<0>();
    if (w == 0 && lane < (HALF ? 8 : 16)) __hip_atomic_store((gu64*)(ws + WS_HL + (rp * 16 + (HALF ? half * 8 : 0) + lane) * 128) + cp, tag, RLX_AGENT);
}

DI void down_out_job(int tp, int cg, const char* wd, const char* wo, char* ws, char* out, char* lds, int w, int lane, unsigned long long tag) {
    constexpr int D = 4;
    const unsigned lds0 = (unsigned)(uintptr_t)lds;
    const int h = lane >> 5;
    const unsigned l16 = lane * 16;
    unsigned wsrc; { const int row = lane >> 3; wsrc = row * 1024 + cg * 128 + (((lane & 7) ^ (((row >> 1) & 1) << 2)) * 16); }
    int woff[2];
    { const int g = (lane >> 4) & 1, q = (lane & 15) >> 2, p = lane & 3, pp = ((p & 1) << 1) | (p >> 1), row = 4 * h + q;
#pragma unroll
      for (int ni = 0; ni < 2; ++ni) { const int col = ni * 32 + g * 16 + 4 * pp; woff[ni] = row * 128 + (((col >> 3) ^ (((row >> 1) & 1) << 2)) * 16) + (col & 7) * 2; } }
    const char* hx = ws + WS_HF + (tp * 224 + w) * 1024;
    const char* wdw = wd + w * 16384;
    const unsigned ring = RFL(lds0 + w * 10240);
    const lds_cptr ringp = (lds_cptr)lds + w * 10240;
    int rot = RFL((cg * 7 / 4 + (tp & 3) * 7 / 2) % 14);
    f32x16 acc[4] = {};
    u32x4 xr[D][2];
    const char* ol = ws + WS_OL + (tp * 8 + w) * 128;
    unsigned long long ofl; unsigned zero = 0;
    lds_cptr rb0 = ringp + woff[0], rb1 = ringp + woff[1];
    asm volatile("" : "+v"(zero), "+v"(rb0), "+v"(rb1), "+v"(wsrc));
    asm volatile("" : "+v"(acc[0]), "+v"(acc[1]), "+v"(acc[2]), "+v"(acc[3]));
    if (w == 0) line_wait<10>(ws + WS_HL + (tp * 8 + cg) * 128, 14, tag, lane);
    wait_vm_bar_lo<0>();
    PRIO_LO();
#define DN_ISSUE(i) do { \
        asm volatile("" : "+s"(rot)); \
        const int p_ = (i) + rot >= 14 ? (i) + rot - 14 : (i) + rot; \
        xr[(i) % D][0] = gld16_dev(hx + p_ * 8192, l16); xr[(i) % D][1] = gld16_dev(hx + p_ * 8192 + 114688, l16); \
        glds(wdw + p_ * 131072, wsrc, ring + ((i) % 5) * 2048); glds(wdw + p_ * 131072 + 8192, wsrc, ring + ((i) % 5) * 2048 + 1024); } while (0)
#define WO_ISSUE(j) do { glds(wo + (w * 4 + (j)) * 16384, wsrc, ring + (((j) + 14) % 5) * 2048); glds(wo + (w * 4 + (j)) * 16384 + 8192, wsrc, ring + (((j) + 14) % 5) * 2048 + 1024); } while (0)
#pragma unroll
    for (int i = 0; i < D; ++i) DN_ISSUE(i);
#pragma unroll
    for (int i = 0; i < 14; ++i) {
        const int full = (i + D - 1 < 14 ? D - 1 : 13 - i), half = D - 1 - full;
        asm volatile("s_waitcnt vmcnt(%2)" : "+v"(xr[i % D][0]), "+v"(xr[i % D][1]) : "i"(4 * full + 2 * half) : "memory");
        const int ro = (i % 5) * 2048;
        const bf16x8 w0 = cat(lds_tr(rb0 + ro), lds_tr(rb0 + ro + 1024)), w1 = cat(lds_tr(rb1 + ro), lds_tr(rb1 + ro + 1024));
        const bf16x8 x0 = __builtin_bit_cast(bf16x8, xr[i % D][0]), x1 = __builtin_bit_cast(bf16x8, xr[i % D][1]);
        acc[0] = MFMA(w0, x0, acc[0]); acc[1] = MFMA(w0, x1, acc[1]); acc[2] = MFMA(w1, x0, acc[2]); acc[3] = MFMA(w1, x1, acc[3]);
        asm volatile("" : "+v"(acc[0]), "+v"(acc[1]), "+v"(acc[2]), "+v"(acc[3]));
        if (i + D < 14) DN_ISSUE(i + D); else WO_ISSUE(i + D - 14);
        if (i == 12) asm volatile("global_load_dwordx2 %0, %1, %2 sc1" : "=v"(ofl) : "v"(zero), "s"(ol) : "memory");
        if (i % 4 == 3) __builtin_amdgcn_s_barrier();
    }
#undef DN_ISSUE
#undef WO_ISSUE
    PRIO_HI();
    lds_ptr wrp = (lds_ptr)lds + w * 10240 + l16;
    lds_cptr rd0 = (lds_cptr)lds + w * 1024 + l16, rd1 = rd0 + (w < 2 ? 8192 : 0);
    unsigned ooff = (tp * 64 + (w & 1) * 32 + (lane & 31)) * 1024 + (cg * 64 + ((w >> 1) & 1) * 32 + (w >> 2) * 16 + 8 * h) * 2;
    asm volatile("s_waitcnt vmcnt(2)" : "+v"(ofl), "+v"(wrp), "+v"(rd0), "+v"(rd1), "+v"(ooff) :: "memory");
    if (!__all(ofl == tag)) line_wait<6>(ol, 1, tag, lane);
    const char* ox = ws + WS_OF + (tp * 64 + w * 4) * 1024;
    u32x4 of[4][2];
#pragma unroll
    for (int i = 0; i < 4; ++i) { of[i][0] = gld16_dev(ox + i * 1024, l16); of[i][1] = gld16_dev(ox + i * 1024 + 32768, l16); }
    asm volatile("s_waitcnt vmcnt(8)" ::: "memory");
    bf16x8 wf[2][2];
#define WO_RD(i) do { const int ro = (((i) + 14) % 5) * 2048; \
        wf[(i) & 1][0] = cat(lds_tr(rb0 + ro), lds_tr(rb0 + ro + 1024)); wf[(i) & 1][1] = cat(lds_tr(rb1 + ro), lds_tr(rb1 + ro + 1024)); } while (0)
    WO_RD(0); WO_RD(1);
#pragma unroll
    for (int i = 0; i < 4; ++i) {
        asm volatile("s_waitcnt vmcnt(%2)" : "+v"(of[i][0]), "+v"(of[i][1]) : "i"(6 - 2 * i) : "memory");
        const bf16x8 x0 = __builtin_bit_cast(bf16x8, of[i][0]), x1 = __builtin_bit_cast(bf16x8, of[i][1]);
        acc[0] = MFMA(wf[i & 1][0], x0, acc[0]); acc[1] = MFMA(wf[i & 1][0], x1, acc[1]); acc[2] = MFMA(wf[i & 1][1], x0, acc[2]); acc[3] = MFMA(wf[i & 1][1], x1, acc[3]);
        asm volatile("" : "+v"(acc[0]), "+v"(acc[1]), "+v"(acc[2]), "+v"(acc[3]));
        if (i < 2) WO_RD(i + 2);
    }
#undef WO_RD
#define RED_ST(slot, j, r) do { const f32x16& a_ = acc[(j) & 3]; const int o_ = ((j) >> 2) * 8 + (r) * 4; lds_st_f4(wrp + (slot) * 1024, (f32x4){a_[o_], a_[o_ + 1], a_[o_ + 2], a_[o_ + 3]}); } while (0)
#pragma unroll
    for (int j = 0; j < 8; ++j) RED_ST(j, j, 0);
    RED_ST(8, 0, 1); RED_ST(9, 1, 1);
    lds_bar_lo();
    f32x4 t[8], u[8];
#pragma unroll
    for (int q = 0; q < 8; ++q) t[q] = lds_f4(rd0 + q * 10240);
    lds_bar();
#pragma unroll
    for (int j = 2; j < 8; ++j) RED_ST(j, j, 1);
#undef RED_ST
    lds_bar();
#pragma unroll
    for (int q = 0; q < 8; ++q) u[q] = lds_f4(rd1 + q * 10240);
    const f32x4 b0 = ((t[0] + t[1]) + (t[2] + t[3])) + ((t[4] + t[5]) + (t[6] + t[7])), b1 = ((u[0] + u[1]) + (u[2] + u[3])) + ((u[4] + u[5]) + (u[6] + u[7]));
    asm volatile("global_store_dwordx4 %0, %1, %2" :: "v"(ooff), "v"(pack44(b0, b1)), "s"(out) : "memory");
}

constexpr int KVT = 2080;
DI float xhalf_max(float m) { auto rr = __builtin_amdgcn_permlane32_swap(__float_as_uint(m), __float_as_uint(m), false, false); return fmaxf(__uint_as_float(rr[0]), __uint_as_float(rr[1])); }
DI float max16(const f32x16& s) {
    float a = fmaxf(fmaxf(s[0], s[1]), s[2]), b = fmaxf(fmaxf(s[3], s[4]), s[5]);
    a = fmaxf(fmaxf(a, s[6]), s[7]); b = fmaxf(fmaxf(b, s[8]), s[9]); a = fmaxf(fmaxf(a, s[10]), s[11]); b = fmaxf(fmaxf(b, s[12]), s[13]);
    a = fmaxf(fmaxf(a, s[14]), s[15]); return fmaxf(a, b);
}
DI u32x4 exp_pack(const f32x16& s, int o) {
    u32x4 p;
#pragma unroll
    for (int i = 0; i < 4; ++i) p[i] = pack_bf16(__builtin_amdgcn_exp2f(s[o + 2 * i]), __builtin_amdgcn_exp2f(s[o + 2 * i + 1]));
    return p;
}
DI f32x16 attn_exact(const char* kv, bf16x8 qf, int lane) {
    f32x16 o = {}; float m = -INFINITY;
#pragma unroll 1
    for (int t = 0; t < 64; ++t) {
        u32x4 kf, v0, v1;
        asm volatile("global_load_dwordx4 %0, %3, %5 sc1\n\tglobal_load_dwordx4 %1, %4, %5 offset:1024 sc1\n\tglobal_load_dwordx4 %2, %4, %5 offset:1536 sc1\n\ts_waitcnt vmcnt(0)"
                     : "=&v"(kf), "=&v"(v0), "=&v"(v1) : "v"(t * 2048 + lane * 16), "v"(t * 2048 + (lane >> 5) * 256 + (lane & 15) * 16), "s"(kv) : "memory");
        if (lane & 16) { const unsigned c = (lane & 11) == 0 ? 0x3F803F80u : 0u; v0 = (u32x4){c, c, c, c}; v1 = v0; }
        f32x16 s = MFMA(__builtin_bit_cast(bf16x8, kf), qf, (f32x16){});
        const float mn = fmaxf(m, xhalf_max(max16(s)));
        const float al = __builtin_amdgcn_exp2f(m - mn);
        m = mn;
#pragma unroll
        for (int i = 0; i < 16; ++i) { o[i] *= al; s[i] -= mn; }
        o = MFMA(__builtin_bit_cast(bf16x8, v0), __builtin_bit_cast(bf16x8, exp_pack(s, 0)), o);
        o = MFMA(__builtin_bit_cast(bf16x8, v1), __builtin_bit_cast(bf16x8, exp_pack(s, 8)), o);
    }
    return o;
}
#define A_E(r) "v_exp_f32 v" #r ", v" #r "\n\t"
#define A_C(d, a, b) "v_cvt_pk_bf16_f32 v" #d ", v" #a ", v" #b "\n\t"
#define A_QK(d, q) "v_mfma_f32_32x32x16_bf16 v[" d "], v[100:103], v[" q "], v[80:95]\n\t"
#define A_PV(o, a, p) "v_mfma_f32_32x32x16_bf16 v[" o "], v[" a "], v[" p "], v[" o "]\n\t"
#define A_W(n) "s_waitcnt lgkmcnt(" #n ")\n\t"
#define A_RD(d, base, u) "ds_read_b128 v[" d "], " base " offset:((" u ")%%16)*4160\n\t"
#define A_U(S0,S1,S2,S3,S4,S5,S6,S7,S8,S9,S10,S11,S12,S13,S14,S15,P14,P15, M1, M2, M3) \
    A_E(S0) A_C(119,P14,P15) A_E(S1) A_E(S2) M1 A_E(S3) A_C(112,S0,S1) A_E(S4) A_E(S5) A_C(113,S2,S3) \
    M2 A_E(S6) A_E(S7) A_C(114,S4,S5) A_E(S8) A_E(S9) A_C(115,S6,S7) A_E(S10) A_E(S11) \
    M3 A_C(116,S8,S9) A_E(S12) A_E(S13) A_C(117,S10,S11) A_E(S14) A_E(S15) A_C(118,S12,S13)
#define A_TA(U) A_U(48,49,50,51,52,53,54,55,56,57,58,59,60,61,62,63,78,79, \
    A_PV("16:31","108:111","116:119") A_RD("108:111", "%11", U), A_QK("64:79","12:15") A_RD("100:103", "%9", U "+1"), A_W(2) A_PV("32:47","104:107","112:115"))
#define A_TB(U) A_U(64,65,66,67,68,69,70,71,72,73,74,75,76,77,78,79,62,63, \
    A_W(0) A_PV("32:47","108:111","116:119"), A_QK("48:63","96:99"), A_PV("16:31","104:107","112:115") A_RD("104:107", "%10", U "+1"))
#define A_OPS "+{v[32:47]}"(oA), "+{v[16:31]}"(oB), "+{v[48:63]}"(sA), "+{v[64:79]}"(sB), "+{v[100:103]}"(kn), "+{v[104:107]}"(v0), "+{v[108:111]}"(v1), "+{v[112:115]}"(p0), "+{v[116:119]}"(p1)
#define SP(n) "s_setprio " #n "\n\t"
#define A_CHUNK(C, BAR, G0, G1, P5, P7) \
    asm volatile(A_TA(#C "*4") A_TB(#C "*4") SP(0) A_TA(#C "*4+1") BAR SP(2) A_TB(#C "*4+1") G0 A_TA(#C "*4+2") G1 P5 A_TB(#C "*4+2") A_TA(#C "*4+3") P7 A_TB(#C "*4+3") \
                 : A_OPS : "v"(bk), "v"(bv0), "v"(bv1), "v"(kvo), "s"(kv + ((C) + 3) * 16384), "s"(dst0 + (((C) + 3) & 3) * 8 * KVT), "{v[80:95]}"(cn), "{v[96:99]}"(qA), "{v[12:15]}"(qB) : "memory")
#define A_BAR(n) "s_waitcnt vmcnt(" #n ")\n\ts_barrier\n\t"
#define A_G0 "s_mov_b32 m0, %14\n\ts_nop 0\n\tglobal_load_lds_dwordx4 %12, %13 sc1\n\t"
#define A_G1 "global_load_lds_dwordx4 %12, %13 offset:1024 sc1\n\t"
DI void attn_role(int blk, const char* x, const char* wq, char* ws, char* lds, int w, int lane, unsigned long long tag) {
    const int xcd = blk & 7;
    const int tp = (xcd >> 1) * 8 + ((blk >> 3) & 7), hg = (xcd & 1) * 4 + (blk >> 6);
    const unsigned lds0 = (unsigned)(uintptr_t)lds;
    const int kh = w >> 2;
    unsigned xs, wsr; int xoff[2], woff;
    { const int row = 8 * w + (lane >> 3);
      xs = (tp * 64 + row) * 1024 + (((lane & 7) ^ ((row >> 1) & 7)) * 16);
      wsr = row * 1024 + hg * 128 + (((lane & 7) ^ (((row >> 1) & 1) << 2)) * 16);
      const int h = lane >> 5, tok = (w & 1) * 32 + (lane & 31), col = ((w >> 1) & 1) * 32 + ((lane >> 4) & 1) * 16 + 4 * (lane & 3), q = (lane & 15) >> 2;
#pragma unroll
      for (int i = 0; i < 2; ++i) xoff[i] = tok * 128 + (((4 * kh + 2 * i + h) ^ ((tok >> 1) & 7)) * 16);
      woff = 8192 + (32 * kh + 8 * h + q) * 128 + (((col >> 3) ^ (((q >> 1) & 1) << 2)) * 16) + (col & 7) * 2; }
    const int qrot = 2 * (hg & 3) + tp;
    if (blk >= 192) __builtin_amdgcn_s_sleep(40);
#pragma unroll
    for (int s = 0; s < 5; ++s) { const unsigned b_ = lds0 + w * 1024 + ((s + 4) % 5) * 16384; const int k_ = (s + qrot) & 7; glds(x + k_ * 128, xs, b_); glds(wq + k_ * 65536, wsr, b_ + 8192); }
    u32x4 qr[3][2];
#pragma unroll
    for (int j = 0; j < 3; ++j) { const int k_ = (5 + j + qrot) & 7; qr[j][0] = gld16_async(x + k_ * 128, xs); qr[j][1] = gld16_async(wq + k_ * 65536, wsr); }
    f32x16 acc = {};
    bf16x8 fx[8], fw[8];
#define Q_RD(s, o) do { const lds_cptr b_ = (lds_cptr)lds + (((s) + 4) % 5) * 16384; _Pragma("unroll") for (int i_ = 0; i_ < 2; ++i_) { \
        fw[(o) + i_] = cat(lds_tr(b_ + woff + i_ * 2048), lds_tr(b_ + woff + i_ * 2048 + 512)); fx[(o) + i_] = lds_b128(b_ + xoff[i_]); } } while (0)
#define Q_MM(a_, b_) do { _Pragma("unroll") for (int i_ = (a_); i_ < (b_); ++i_) acc = MFMA(fw[i_], fx[i_], acc); asm volatile("" : "+v"(acc)); } while (0)
    wait_vm_bar<6>();
    SBAR(); Q_RD(0, 0); Q_RD(1, 2); Q_RD(2, 4); Q_RD(3, 6); SBAR();
    Q_MM(0, 2); SBAR(); Q_RD(4, 0); SBAR(); Q_MM(2, 4); SBAR();
    asm volatile("s_waitcnt vmcnt(0) lgkmcnt(0)\n\ts_barrier" : "+v"(qr[0][0]), "+v"(qr[0][1]), "+v"(qr[1][0]), "+v"(qr[1][1]), "+v"(qr[2][0]), "+v"(qr[2][1]) :: "memory");
#pragma unroll
    for (int j = 0; j < 3; ++j) { lds_ptr p_ = (lds_ptr)lds + w * 1024 + lane * 16 + ((j + 9) % 5) * 16384; lds_st_u4(p_, qr[j][0]); lds_st_u4(p_ + 8192, qr[j][1]); }
    SBAR();
    Q_MM(4, 8); SBAR();
    lds_bar();
    SBAR(); Q_RD(5, 2); Q_RD(6, 4); Q_RD(7, 6); SBAR();
    Q_MM(0, 8); SBAR();
#undef Q_RD
#undef Q_MM
#pragma unroll
    for (int i = 0; i < 4; ++i) lds_st_f4((lds_ptr)lds + 32768 + w * 4096 + i * 1024 + lane * 16, (f32x4){acc[4 * i], acc[4 * i + 1], acc[4 * i + 2], acc[4 * i + 3]});
    lds_bar();
#pragma unroll
    for (int i = 0; i < 4; ++i) { const f32x4 v = lds_f4((lds_cptr)lds + 32768 + (w ^ 4) * 4096 + i * 1024 + lane * 16);
#pragma unroll
        for (int j = 0; j < 4; ++j) acc[4 * i + j] += v[j]; }
    const u32x4 q0 = pack8(acc, 0, QSCALE), q1 = pack8(acc, 1, QSCALE);
    bf16x8 qA = __builtin_bit_cast(bf16x8, kh ? q0 : q1), qB = __builtin_bit_cast(bf16x8, kh ? q1 : q0);
    const char* kv = ws + WS_KV;
    unsigned kvo = w * 2048 + lane * 16, goff = (tp * 64 + (w & 1) * 32 + (lane & 31)) * 4;
    const unsigned dst0 = RFL(lds0 + w * KVT);
    unsigned bk = lds0 + kh * KVT + lane * 16;
    const unsigned pad = lds0 + 2048 + ((lane & 11) == 0 ? 0 : 16);
    unsigned bv0 = (lane & 16) ? pad : lds0 + kh * KVT + 1024 + (lane >> 5) * 256 + (lane & 15) * 16;
    unsigned bv1 = (lane & 16) ? pad : bv0 + 512;
    f32x16 oA = {}, oB = {}, sB = {};
    u32x4 p0 = {0u, 0u, 0u, 0u}, p1 = {0u, 0u, 0u, 0u};
    bf16x8 v1 = {};
    asm volatile("" : "+{v[96:99]}"(qA), "+{v[12:15]}"(qB), "+v"(kvo), "+v"(goff), "+v"(bk), "+v"(bv0), "+v"(bv1), "+{v[32:47]}"(oA), "+{v[16:31]}"(oB), "+{v[112:115]}"(p0), "+{v[116:119]}"(p1), "+{v[108:111]}"(v1));
    if (w == 0) line_wait<15>(ws + WS_KVF + (blk & (NREP - 1)) * 512, 64, tag, lane);
    PRIO_LO();
    lds_bar();
#pragma unroll
    for (int c = 0; c < 4; ++c) { glds_dev(kv + c * 16384, kvo, dst0 + c * 8 * KVT); glds_dev(kv + c * 16384 + 1024, kvo, dst0 + c * 8 * KVT + 1024); }
    unsigned gu; asm volatile("global_load_dword %0, %1, %2 sc1" : "=v"(gu) : "v"(goff), "s"(ws + WS_G) : "memory");
    if (w == 1) { const unsigned c = (lane & 1) ? 0u : 0x3F803F80u; lds_st_u4((lds_ptr)lds + (lane >> 1) * KVT + 2048 + (lane & 1) * 16, (u32x4){c, c, c, c}); }
    wait_vm_bar<7>();
    f32x16 cn, sA;
    bf16x8 kn = lds_b128((lds_cptr)(uintptr_t)bk);
    bf16x8 v0 = lds_b128((lds_cptr)(uintptr_t)bv0);
    { const bf16x8 k0 = lds_b128((lds_cptr)lds + lane * 16);
      sA = MFMA(k0, qA, (f32x16){}); sB = MFMA(k0, qB, (f32x16){});
      const float m = xhalf_max(fmaxf(max16(sA), max16(sB)));
#pragma unroll
      for (int i = 0; i < 16; ++i) cn[i] = -m; }
    sA = MFMA(kn, qA, cn); sB[14] = 0.f; sB[15] = 0.f;
    unsigned m0save; asm volatile("s_mov_b32 %0, m0\n\ts_nop 15" : "=s"(m0save), A_OPS);
    A_CHUNK(0, A_BAR(5), "", "", "", SP(1));
    A_CHUNK(1, A_BAR(3), A_G0, A_G1, "", SP(1));
    A_CHUNK(2, A_BAR(2), A_G0, A_G1, "", SP(1));
    A_CHUNK(3, A_BAR(2), A_G0, A_G1, "", SP(1));
    A_CHUNK(4, A_BAR(2), A_G0, A_G1, "", SP(1));
    A_CHUNK(5, A_BAR(2), "", "", "", SP(1));
    A_CHUNK(6, A_BAR(0), "", "", "", SP(1));
    A_CHUNK(7, "s_barrier\n\t", "", "", SP(1), SP(0));
    asm volatile("s_mov_b32 m0, %1" : "+v"(gu) : "s"(m0save));
    asm volatile("s_setprio 3\n\t" A_C(119,78,79) "s_waitcnt lgkmcnt(0)\n\ts_nop 1\n\t" A_PV("16:31","108:111","116:119") "s_nop 15" : A_OPS :: "memory");
    int ln; asm volatile("v_mbcnt_lo_u32_b32 %0, -1, 0\n\tv_mbcnt_hi_u32_b32 %0, -1, %0" : "=v"(ln));
    f32x16 o = oB;
    { lds_st_f4((lds_ptr)lds + w * 3072 + ln * 16, (f32x4){oA[0], oA[1], oA[2], oA[3]}); lds_st_f4((lds_ptr)lds + w * 3072 + 1024 + ln * 16, (f32x4){oA[4], oA[5], oA[6], oA[7]});
      lds_st_f((lds_ptr)lds + w * 3072 + 2048 + ln * 4, oA[8]);
      lds_bar();
      const f32x4 a0 = lds_f4((lds_cptr)lds + (w ^ 4) * 3072 + ln * 16), a1 = lds_f4((lds_cptr)lds + (w ^ 4) * 3072 + 1024 + ln * 16);
#pragma unroll
      for (int j = 0; j < 4; ++j) { o[j] += a0[j]; o[4 + j] += a1[j]; }
      o[8] += lds_f((lds_cptr)lds + (w ^ 4) * 3072 + 2048 + ln * 4); }
    float mx = fabsf(o[0]);
#pragma unroll
    for (int i = 1; i < 8; ++i) mx = fmaxf(mx, fabsf(o[i]));
    const bool ok = (o[8] < 1.0e18f) && (o[8] > 1.0e-18f) && (mx < 1.0e36f);
    if (__builtin_expect(!__all(ok), 0)) o = attn_exact(kv, qB, ln);
    const float inv = __uint_as_float(gu) * __builtin_amdgcn_rcpf(o[8]);
    gst16_dev(ws + WS_OF + ((tp * 2 + (w & 1)) * 32 + hg * 4 + ((w >> 1) & 1) * 2 + (w >> 2)) * 1024, ln * 16, pack8(o, 0, inv));
    wait_vm_bar<0>();
    if (w == 0 && ln == 0) __hip_atomic_store((gu64*)(ws + WS_OL + (tp * 8 + hg) * 128), tag, RLX_AGENT);
}

__global__ __launch_bounds__(512, 4) void k_all(const char* __restrict__ x, const char* __restrict__ wq, const char* __restrict__ wk, const char* __restrict__ wv, const char* __restrict__ wo,
                                                const char* __restrict__ wg, const char* __restrict__ wu, const char* __restrict__ wd, const char* __restrict__ gw,
                                                char* __restrict__ ws, char* __restrict__ out) {
    extern __shared__ __attribute__((aligned(16))) char lds[];
    const int tid = threadIdx.x, lane = tid & 63; const int w = RFL(tid >> 6);
    const int b = blockIdx.x;
    const unsigned long long tag = (llvm_dispatch_id() + 1) * 0x9E3779B97F4A7C15ull + (unsigned long long)__builtin_amdgcn_dispatch_ptr();
    if (b < 256) {
        attn_role(b, x, wq, ws, lds, w, lane, tag);
    } else {
        const int g = b & 255, xcd = g & 7, i = g >> 3;
        if (i >= 24) kv_job(8 * xcd + (i - 24), x, wk, wv, gw, ws, lds, w, lane, tag);
        if (i < 24) up_job<false>(4 * (xcd >> 1) + (i & 3), 7 * (xcd & 1) + (i >> 2), 0, x, wg, wu, ws, lds, w, lane, tag);
        else up_job<true>(4 * (xcd >> 1) + (i & 3), 7 * (xcd & 1) + 6, (i >> 2) & 1, x, wg, wu, ws, lds, w, lane, tag);
        down_out_job(4 * xcd + (i >> 3), i & 7, wd, wo, ws, out, lds, w, lane, tag);
    }
}

extern "C" void kernel_launch(void* const* d_in, const int* in_sizes, int n_in, void* d_out, int out_size, void* d_ws, size_t ws_size, hipStream_t stream) {
    (void)hipFuncSetAttribute((const void*)k_all, hipFuncAttributeMaxDynamicSharedMemorySize, 81920);
    k_all<<<512, 512, 81920, stream>>>((const char*)d_in[0], (const char*)d_in[1], (const char*)d_in[2], (const char*)d_in[3], (const char*)d_in[4],
                                       (const char*)d_in[6], (const char*)d_in[7], (const char*)d_in[8], (const char*)d_in[9], (char*)d_ws, (char*)d_out);
}
```
